```python
import torch

def _softmax(x, axis=-1):
    return torch.softmax(x, dim=axis)
def _gelu(x):
    return torch.nn.functional.gelu(x, approximate="tanh")
def _silu(x):
    return torch.nn.functional.silu(x)
def _sigmoid(x):
    return torch.sigmoid(x)
def _layernorm(x, axis=-1, eps=1e-5):
    m = x.mean(dim=axis, keepdim=True)
    v = x.var(dim=axis, keepdim=True, unbiased=False)
    return (x - m) / torch.sqrt(v + eps)
def _scatter_add(tgt, idx, src, axis=0):
    return tgt.clone().index_add_(axis, idx, src)
def _segment_sum(src, seg, n):
    out = torch.zeros((n,) + src.shape[1:], dtype=src.dtype)
    return out.index_add_(0, seg, src)
def _pad(x, pw):
    flat = tuple(v for pair in reversed(pw) for v in pair)
    return torch.nn.functional.pad(x, flat)

kernel_name = 'standalone__weight_only_int_gptq__prompt'

def setup_inputs():

    _w = (lambda a: (lambda t: t.to(torch.bfloat16) if t.is_floating_point() else t)(torch.as_tensor(a)))
    B, S, D, F = 1, 2048, 1024, 3584
    bits = max(2, min(4, 4))
    pf = 32 // bits
    G = 64 if 64 > 0 else D
    G = max(pf, (G // pf) * pf)
    D = max(G, (D // G) * G)
    ng = D // G
    qmax = 1 << bits
    qw = torch.randint(0, qmax, (D, F)).to(torch.int64)
    packed = torch.zeros((D // pf, F), dtype=torch.int64)
    for i in range(pf):
        packed |= (qw[i::pf] << (i * bits))
    packed = packed.to(torch.int32)
    return {
        "x": _w((lambda *s: torch.randn(*s, dtype=torch.bfloat16) * 0.05)(B, S, D)),
        "qw_packed": _w(packed),
        "scales": _w(torch.abs((lambda *s: torch.randn(*s, dtype=torch.bfloat16) * 0.05)(ng, F)).to(float) * 0.1 + 0.01),
        "zeros": _w(torch.randint(0, qmax, (ng, F)).to(float)),
        "bits": _w((lambda _x: torch.as_tensor(_x, dtype=torch.int32))(bits)),
    }

def reference(x, qw_packed, scales, zeros, bits):
    b = int(bits)
    pf = 32 // b
    mask = (1 << b) - 1
    P, F = qw_packed.shape
    D = P * pf
    ng = scales.shape[0]
    G = D // ng
    shifts = (lambda x, dt: x.to(dt))(torch.arange(pf) * b, qw_packed.dtype)
    qw = ((qw_packed[:, None, :] >> shifts[None, :, None]) & mask).reshape(D, F)
    s = (lambda x, n, axis: torch.repeat_interleave(x, n, dim=axis))(scales, G, axis=0)
    z = (lambda x, n, axis: torch.repeat_interleave(x, n, dim=axis))(zeros, G, axis=0)
    w = ((lambda x, dt: x.to(dt))(qw, x.dtype) - z) * s
    return torch.einsum("bsd,df->bsf", x, w)
```

```cpp
#include <hip/hip_runtime.h>
#include <hip/hip_bf16.h>
#include <stdint.h>

typedef __attribute__((ext_vector_type(8))) short bf16x8_t;
typedef __attribute__((ext_vector_type(8))) __bf16 bf16x8;
typedef __attribute__((ext_vector_type(16))) float f32x16;
typedef __attribute__((ext_vector_type(4))) unsigned u32x4;
typedef __attribute__((ext_vector_type(2))) float f32x2;
typedef __attribute__((ext_vector_type(2))) __bf16 bf16x2;

constexpr int M_ = 2048, N_ = 3584, K_ = 1024, G_ = 64;
constexpr int BM = 256, BN = 128, BK = 64;

__device__ __forceinline__ unsigned cvtpk(float lo, float hi) {
  f32x2 v = {lo, hi}; bf16x2 b = __builtin_convertvector(v, bf16x2); return __builtin_bit_cast(unsigned, b);
}
__device__ __forceinline__ float mixlo(unsigned t, float s, float c) {
  float r; asm("v_fma_mix_f32 %0, %1, %2, %3 op_sel_hi:[1,0,0]" : "=v"(r) : "v"(t), "v"(s), "v"(c)); return r;
}
__device__ __forceinline__ float mixhi(unsigned t, float s, float c) {
  float r; asm("v_fma_mix_f32 %0, %1, %2, %3 op_sel:[1,0,0] op_sel_hi:[1,0,0]" : "=v"(r) : "v"(t), "v"(s), "v"(c)); return r;
}
__device__ __forceinline__ bf16x8 dequant(unsigned p, float s24, float s20, float c) {
  unsigned t0 = p & 0x000F000Fu;
  unsigned t1 = p & 0x00F000F0u;
  unsigned ph = p >> 8;
  unsigned t2 = ph & 0x000F000Fu;
  unsigned t3 = ph & 0x00F000F0u;
  u32x4 r;
  r[0] = cvtpk(mixlo(t0, s24, c), mixlo(t1, s20, c));
  r[1] = cvtpk(mixlo(t2, s24, c), mixlo(t3, s20, c));
  r[2] = cvtpk(mixhi(t0, s24, c), mixhi(t1, s20, c));
  r[3] = cvtpk(mixhi(t2, s24, c), mixhi(t3, s20, c));
  return __builtin_bit_cast(bf16x8, r);
}

__global__ __launch_bounds__(512, 2)
void gptq_gemm_v0(const uint16_t* __restrict__ x, const unsigned* __restrict__ qw,
                  const uint16_t* __restrict__ sc, const uint16_t* __restrict__ zr,
                  uint16_t* __restrict__ out) {
  extern __shared__ __attribute__((aligned(16))) char smem[];
  const int tid = threadIdx.x, lane = tid & 63, wid = tid >> 6;
  const int wm = wid >> 2, wn = wid & 3;
  const int bid = blockIdx.x;
  const int pm = bid & 7, pn = bid >> 3;
  const int m0 = pm * BM, n0 = pn * BN;
  const int r = lane & 31, h = lane >> 5;
  const int ncol = n0 + wn * 32 + (16 * ((r >> 2) & 1) + (r & 3) + 4 * (r >> 3));

  f32x16 acc[4];
#pragma unroll
  for (int i = 0; i < 4; ++i)
#pragma unroll
    for (int j = 0; j < 16; ++j) acc[i][j] = 0.f;

  auto stage = [&](int buf, int kt) {
#pragma unroll
    for (int i = 0; i < 4; ++i) {
      int piece = wid * 4 + i;
      int row = piece * 8 + (lane >> 3);
      int ch = (lane & 7) ^ ((row >> 1) & 7);
      const uint16_t* src = x + (size_t)(m0 + row) * K_ + kt * BK + ch * 8;
      __builtin_amdgcn_global_load_lds((const __attribute__((address_space(1))) void*)src,
          (__attribute__((address_space(3))) void*)(smem + buf * 32768 + piece * 1024), 16, 0, 0);
    }
  };
  constexpr int NT = K_ / BK;
  stage(0, 0);
  unsigned wq[4];
#pragma unroll
  for (int ks = 0; ks < 4; ++ks) wq[ks] = qw[(size_t)(2 * ks + h) * N_ + ncol];
  float sf = __uint_as_float((unsigned)sc[ncol] << 16), zf = __uint_as_float((unsigned)zr[ncol] << 16);
  __syncthreads();
  for (int kt = 0; kt < NT; ++kt) {
    const int buf = kt & 1;
    unsigned wqn[4]; float sfn = 0.f, zfn = 0.f;
    if (kt + 1 < NT) {
      stage(buf ^ 1, kt + 1);
#pragma unroll
      for (int ks = 0; ks < 4; ++ks) wqn[ks] = qw[(size_t)((kt + 1) * 8 + 2 * ks + h) * N_ + ncol];
      sfn = __uint_as_float((unsigned)sc[(kt + 1) * N_ + ncol] << 16);
      zfn = __uint_as_float((unsigned)zr[(kt + 1) * N_ + ncol] << 16);
    }
    const float s24 = sf * 16777216.f, s20 = sf * 1048576.f, c = -zf * sf;
#pragma unroll
    for (int ks = 0; ks < 4; ++ks) {
      bf16x8 wf = dequant(wq[ks], s24, s20, c);
#pragma unroll
      for (int mt = 0; mt < 4; ++mt) {
        int row = wm * 128 + mt * 32 + r;
        int ch = (2 * ks + h) ^ ((row >> 1) & 7);
        bf16x8 xf = *reinterpret_cast<const bf16x8*>(smem + buf * 32768 + row * 128 + ch * 16);
        acc[mt] = __builtin_amdgcn_mfma_f32_32x32x16_bf16(wf, xf, acc[mt], 0, 0, 0);
      }
    }
    __syncthreads();
#pragma unroll
    for (int ks = 0; ks < 4; ++ks) wq[ks] = wqn[ks];
    sf = sfn; zf = zfn;
  }
#pragma unroll
  for (int mt = 0; mt < 4; ++mt) {
    int m = m0 + wm * 128 + mt * 32 + r;
    u32x4 a, b;
#pragma unroll
    for (int j = 0; j < 4; ++j) { a[j] = cvtpk(acc[mt][2 * j], acc[mt][2 * j + 1]); b[j] = cvtpk(acc[mt][8 + 2 * j], acc[mt][8 + 2 * j + 1]); }
    u32x4* dst = reinterpret_cast<u32x4*>(out + (size_t)m * N_ + n0 + wn * 32 + 16 * h);
    dst[0] = a; dst[1] = b;
  }
}

extern "C" void kernel_launch(void* const* d_in, const int* in_sizes, int n_in,
                              void* d_out, int out_size, void* d_ws, size_t ws_size,
                              hipStream_t stream) {
  const uint16_t* x = (const uint16_t*)d_in[0];
  const unsigned* qw = (const unsigned*)d_in[1];
  const uint16_t* sc = (const uint16_t*)d_in[2];
  const uint16_t* zr = (const uint16_t*)d_in[3];
  hipFuncSetAttribute((const void*)gptq_gemm_v0, hipFuncAttributeMaxDynamicSharedMemorySize, 65536);
  gptq_gemm_v0<<<224, 512, 65536, stream>>>(x, qw, sc, zr, (uint16_t*)d_out);
}
```

```cpp
#include <hip/hip_runtime.h>
#include <stdint.h>

constexpr int LDS_BYTES = 163840;
constexpr int NBLK = 252;

__global__ __launch_bounds__(256, 1)
void gptq_gemm(const uint16_t* __restrict__ x, const unsigned* __restrict__ qw,
               const uint16_t* __restrict__ sc, const uint16_t* __restrict__ zr,
               uint16_t* __restrict__ out) {
  const auto ka = __builtin_amdgcn_kernarg_segment_ptr();
  const unsigned bid = blockIdx.x;
  const unsigned tid = threadIdx.x;
  asm volatile(
      "s_load_dwordx2 s[24:25], %[ka], 0x8\n\t"
      "s_load_dwordx2 s[28:29], %[ka], 0x10\n\t"
      "s_load_dwordx2 s[4:5], %[ka], 0x18\n\t"
      "s_load_dwordx2 s[20:21], %[ka], 0x0\n\t"
      "s_load_dwordx2 s[36:37], %[ka], 0x20\n\t"
      "s_getpc_b64 s[96:97]\n\t"
      "LPC%=:\n\t"
      "s_bfe_u32 s9, %[bid], 0x30003\n\t"
      "s_lshl_b32 s9, s9, 12\n\t"
      "v_min_u32 v160, 31, %[tid]\n\t"
      "v_lshl_add_u32 v160, v160, 7, s9\n\t"
      "v_min_u32 v160, LEND%=-LPC%=-4, v160\n\t"
      "global_load_dword v161, v160, s[96:97]\n\t"
      "v_readfirstlane_b32 s8, %[tid]\n\t"
      "s_and_b32 s9, %[bid], 7\n\t"
      "s_lshr_b32 s10, %[bid], 3\n\t"
      "s_mul_i32 s11, s9, 31\n\t"
      "s_min_u32 s12, s9, 4\n\t"
      "s_add_u32 s11, s11, s12\n\t"
      "s_add_u32 s11, s11, s10\n\t"
      "s_mul_i32 s12, s11, 2341\n\t"
      "s_lshr_b32 s12, s12, 16\n\t"
      "s_mul_i32 s13, s12, 28\n\t"
      "s_sub_u32 s13, s11, s13\n\t"
      "s_mul_i32 s14, s12, 224\n\t"
      "s_mul_i32 s16, s12, 7\n\t"
      "s_sub_i32 s17, s16, 31\n\t"
      "s_max_i32 s17, s17, 0\n\t"
      "s_add_u32 s15, s14, 224\n\t"
      "s_add_u32 s14, s14, s17\n\t"
      "s_sub_i32 s17, s16, 24\n\t"
      "s_max_i32 s17, s17, 0\n\t"
      "s_add_u32 s15, s15, s17\n\t"
      "s_mov_b32 s18, s13\n\t"
      "s_and_b32 s18, s18, 1\n\t"
      "s_lshl_b32 s18, s18, 10\n\t"
      "s_lshr_b32 s8, s8, 6\n\t"
      "s_lshl_b32 s13, s13, 7\n\t"
      "s_lshl_b32 s16, s8, 5\n\t"
      "s_add_u32 s13, s13, s16\n\t"
      "s_lshl_b32 s19, s8, 13\n\t"
      "s_lshl_b32 s22, s15, 11\n\t"
      "s_mov_b32 s23, 0x20000\n\t"
      "s_mov_b32 s27, s23\n\t"
      "s_mov_b32 s31, s23\n\t"
      "s_mov_b32 s7, s23\n\t"
      "s_mov_b32 s26, 1835008\n\t"
      "s_mov_b32 s30, 114688\n\t"
      "s_mov_b32 s6, s30\n\t"
      "v_and_b32 v136, 15, %[tid]\n\t"
      "v_bfe_u32 v137, %[tid], 4, 2\n\t"
      "v_lshl_add_u32 v138, v136, 1, s13\n\t"
      "v_lshlrev_b32 v15, 1, v138\n\t"
      "v_mul_u32_u24 v139, 3584, v137\n\t"
      "v_add_lshl_u32 v13, v139, v138, 2\n\t"
      "v_add_u32 v14, 57344, v13\n\t"
      "s_lshl_b32 s16, s8, 6\n\t"
      "s_add_u32 s16, s16, s14\n\t"
      "s_lshl_b32 s16, s16, 11\n\t"
      "v_and_b32 v1, 7, %[tid]\n\t"
      "v_xor_b32 v1, v1, v137\n\t"
      "v_lshlrev_b32 v1, 4, v1\n\t"
      "v_bfe_u32 v2, %[tid], 3, 3\n\t"
      "v_lshl_or_b32 v1, v2, 11, v1\n\t"
      "v_xor_b32 v2, 64, v1\n\t"
      "v_add_u32 v1, s16, v1\n\t"
      "v_add_u32 v2, s16, v2\n\t"
      "v_add_u32 v8, 111616, v2\n\t"
      "v_add_u32 v7, 96256, v1\n\t"
      "v_add_u32 v6, 80896, v2\n\t"
      "v_add_u32 v5, 65536, v1\n\t"
      "v_add_u32 v4, 46080, v2\n\t"
      "v_add_u32 v3, 30720, v1\n\t"
      "v_add_u32 v2, 15360, v2\n\t"
      "s_mov_b32 s40, s18\n\t"
      "s_mul_i32 s56, s40, 896\n\t"
      "s_mul_i32 s72, s40, 56\n\t"
      "s_add_u32 s41, s18, 128\n\t"
      "s_and_b32 s41, s41, 0x7ff\n\t"
      "s_mul_i32 s57, s41, 896\n\t"
      "s_mul_i32 s73, s41, 56\n\t"
      "s_add_u32 s42, s18, 256\n\t"
      "s_and_b32 s42, s42, 0x7ff\n\t"
      "s_mul_i32 s58, s42, 896\n\t"
      "s_mul_i32 s74, s42, 56\n\t"
      "s_waitcnt lgkmcnt(0)\n\t"
      "buffer_load_dword v112, v15, s[28:31], s72 offen\n\t"
      "buffer_load_dword v113, v15, s[4:7], s72 offen\n\t"
      "buffer_load_dwordx2 v[96:97], v13, s[24:27], s56 offen\n\t"
      "buffer_load_dwordx2 v[98:99], v14, s[24:27], s56 offen\n\t"
      "buffer_load_dword v114, v15, s[28:31], s73 offen\n\t"
      "buffer_load_dword v115, v15, s[4:7], s73 offen\n\t"
      "s_add_u32 m0, s19, 0\n\t"
      "s_nop 0\n\t"
      "buffer_load_dwordx4 v1, s[20:23], s40 offen offset:0 lds\n\t"
      "buffer_load_dwordx4 v2, s[20:23], s40 offen offset:1024 lds\n\t"
      "buffer_load_dwordx4 v3, s[20:23], s40 offen offset:2048 lds\n\t"
      "buffer_load_dwordx4 v4, s[20:23], s40 offen offset:3072 lds\n\t"
      "s_add_u32 m0, s19, 4096\n\t"
      "s_nop 0\n\t"
      "buffer_load_dwordx4 v5, s[20:23], s40 offen offset:0 lds\n\t"
      "buffer_load_dwordx4 v6, s[20:23], s40 offen offset:1024 lds\n\t"
      "buffer_load_dwordx4 v7, s[20:23], s40 offen offset:2048 lds\n\t"
      "buffer_load_dwordx4 v8, s[20:23], s40 offen offset:3072 lds\n\t"
      "v_mov_b32 v10, 0xffff0000\n\t"
      "v_mov_b32 v11, 0x000f000f\n\t"
      "v_mov_b32 v12, 0x00f000f0\n\t"
      "v_lshrrev_b32 v152, 1, v136\n\t"
      "v_xor_b32 v152, v152, v137\n\t"
      "v_lshlrev_b32 v152, 4, v152\n\t"
      "v_lshl_or_b32 v152, v136, 7, v152\n\t"
      "v_xor_b32 v153, 64, v152\n\t"
      "v_add_u32 v154, 0x10000, v152\n\t"
      "v_add_u32 v155, 0x10000, v153\n\t"
      "v_add_u32 v156, 0x20000, v152\n\t"
      "v_add_u32 v157, 0x20000, v153\n\t"
      "s_mul_i32 s99, s14, 7168\n\t"
      "s_mul_i32 s38, s15, 7168\n\t"
      "s_mov_b32 s39, s23\n\t"
      "s_add_u32 s43, s18, 384\n\t"
      "s_and_b32 s43, s43, 0x7ff\n\t"
      "s_mul_i32 s59, s43, 896\n\t"
      "s_mul_i32 s75, s43, 56\n\t"
      "s_add_u32 s44, s18, 512\n\t"
      "s_and_b32 s44, s44, 0x7ff\n\t"
      "s_mul_i32 s60, s44, 896\n\t"
      "s_mul_i32 s76, s44, 56\n\t"
      "s_add_u32 s45, s18, 640\n\t"
      "s_and_b32 s45, s45, 0x7ff\n\t"
      "s_mul_i32 s61, s45, 896\n\t"
      "s_mul_i32 s77, s45, 56\n\t"
      "s_add_u32 s46, s18, 768\n\t"
      "s_and_b32 s46, s46, 0x7ff\n\t"
      "s_mul_i32 s62, s46, 896\n\t"
      "s_mul_i32 s78, s46, 56\n\t"
      "s_add_u32 s47, s18, 896\n\t"
      "s_and_b32 s47, s47, 0x7ff\n\t"
      "s_mul_i32 s63, s47, 896\n\t"
      "s_mul_i32 s79, s47, 56\n\t"
      "s_add_u32 s48, s18, 1024\n\t"
      "s_and_b32 s48, s48, 0x7ff\n\t"
      "s_mul_i32 s64, s48, 896\n\t"
      "s_mul_i32 s80, s48, 56\n\t"
      "s_add_u32 s49, s18, 1152\n\t"
      "s_and_b32 s49, s49, 0x7ff\n\t"
      "s_mul_i32 s65, s49, 896\n\t"
      "s_mul_i32 s81, s49, 56\n\t"
      "s_add_u32 s50, s18, 1280\n\t"
      "s_and_b32 s50, s50, 0x7ff\n\t"
      "s_mul_i32 s66, s50, 896\n\t"
      "s_mul_i32 s82, s50, 56\n\t"
      "s_add_u32 s51, s18, 1408\n\t"
      "s_and_b32 s51, s51, 0x7ff\n\t"
      "s_mul_i32 s67, s51, 896\n\t"
      "s_mul_i32 s83, s51, 56\n\t"
      "s_add_u32 s52, s18, 1536\n\t"
      "s_and_b32 s52, s52, 0x7ff\n\t"
      "s_mul_i32 s68, s52, 896\n\t"
      "s_mul_i32 s84, s52, 56\n\t"
      "s_add_u32 s53, s18, 1664\n\t"
      "s_and_b32 s53, s53, 0x7ff\n\t"
      "s_mul_i32 s69, s53, 896\n\t"
      "s_mul_i32 s85, s53, 56\n\t"
      "s_add_u32 s54, s18, 1792\n\t"
      "s_and_b32 s54, s54, 0x7ff\n\t"
      "s_mul_i32 s70, s54, 896\n\t"
      "s_mul_i32 s86, s54, 56\n\t"
      "s_add_u32 s55, s18, 1920\n\t"
      "s_and_b32 s55, s55, 0x7ff\n\t"
      "s_mul_i32 s71, s55, 896\n\t"
      "s_mul_i32 s87, s55, 56\n\t"
      "s_waitcnt vmcnt(11)\n\t"
      "buffer_load_dwordx2 v[100:101], v13, s[24:27], s57 offen\n\t"
      "buffer_load_dwordx2 v[102:103], v14, s[24:27], s57 offen\n\t"
      "buffer_load_dword v116, v15, s[28:31], s74 offen\n\t"
      "buffer_load_dword v117, v15, s[4:7], s74 offen\n\t"
      "v_lshlrev_b32 v120, 16, v112\n\t"
      "v_and_b32 v121, v10, v112\n\t"
      "v_lshlrev_b32 v122, 16, v113\n\t"
      "v_and_b32 v123, v10, v113\n\t"
      "s_add_u32 m0, s19, 32768\n\t"
      "s_nop 0\n\t"
      "buffer_load_dwordx4 v1, s[20:23], s41 offen offset:0 lds\n\t"
      "v_mul_f32 v124, 0x4b800000, v120\n\t"
      "v_mul_f32 v126, 0x49800000, v120\n\t"
      "v_mul_f32_e64 v128, -v122, v120\n\t"
      "v_mul_f32 v125, 0x4b800000, v121\n\t"
      "buffer_load_dwordx4 v2, s[20:23], s41 offen offset:1024 lds\n\t"
      "v_mul_f32 v127, 0x49800000, v121\n\t"
      "v_mul_f32_e64 v129, -v123, v121\n\t"
      "v_lshrrev_b32 v139, 8, v96\n\t"
      "v_lshrrev_b32 v143, 8, v97\n\t"
      "buffer_load_dwordx4 v3, s[20:23], s41 offen offset:2048 lds\n\t"
      "v_and_b32 v136, v11, v96\n\t"
      "v_and_b32 v137, v12, v96\n\t"
      "v_and_b32 v140, v11, v97\n\t"
      "v_and_b32 v141, v12, v97\n\t"
      "buffer_load_dwordx4 v4, s[20:23], s41 offen offset:3072 lds\n\t"
      "v_and_b32 v138, v11, v139\n\t"
      "v_and_b32 v139, v12, v139\n\t"
      "v_and_b32 v142, v11, v143\n\t"
      "v_and_b32 v143, v12, v143\n\t"
      "s_add_u32 m0, s19, 36864\n\t"
      "s_nop 0\n\t"
      "buffer_load_dwordx4 v5, s[20:23], s41 offen offset:0 lds\n\t"
      "v_fma_mix_f32 v144, v136, v124, v128 op_sel_hi:[1,0,0]\n\t"
      "v_fma_mix_f32 v145, v137, v126, v128 op_sel_hi:[1,0,0]\n\t"
      "v_fma_mix_f32 v146, v138, v124, v128 op_sel_hi:[1,0,0]\n\t"
      "v_fma_mix_f32 v147, v139, v126, v128 op_sel_hi:[1,0,0]\n\t"
      "buffer_load_dwordx4 v6, s[20:23], s41 offen offset:1024 lds\n\t"
      "v_fma_mix_f32 v148, v136, v124, v128 op_sel:[1,0,0] op_sel_hi:[1,0,0]\n\t"
      "v_fma_mix_f32 v149, v137, v126, v128 op_sel:[1,0,0] op_sel_hi:[1,0,0]\n\t"
      "v_cvt_pk_bf16_f32 v16, v144, v145\n\t"
      "v_cvt_pk_bf16_f32 v17, v146, v147\n\t"
      "buffer_load_dwordx4 v7, s[20:23], s41 offen offset:2048 lds\n\t"
      "v_fma_mix_f32 v150, v138, v124, v128 op_sel:[1,0,0] op_sel_hi:[1,0,0]\n\t"
      "v_fma_mix_f32 v151, v139, v126, v128 op_sel:[1,0,0] op_sel_hi:[1,0,0]\n\t"
      "v_cvt_pk_bf16_f32 v18, v148, v149\n\t"
      "v_cvt_pk_bf16_f32 v19, v150, v151\n\t"
      "buffer_load_dwordx4 v8, s[20:23], s41 offen offset:3072 lds\n\t"
      "v_fma_mix_f32 v144, v140, v125, v129 op_sel_hi:[1,0,0]\n\t"
      "v_fma_mix_f32 v145, v141, v127, v129 op_sel_hi:[1,0,0]\n\t"
      "v_fma_mix_f32 v146, v142, v125, v129 op_sel_hi:[1,0,0]\n\t"
      "v_fma_mix_f32 v147, v143, v127, v129 op_sel_hi:[1,0,0]\n\t"
      "buffer_load_dword v36, v15, s[28:31], s84 offen\n\t"
      "buffer_load_dword v37, v15, s[4:7], s84 offen\n\t"
      "buffer_load_dwordx2 v[38:39], v13, s[24:27], s68 offen\n\t"
      "buffer_load_dwordx2 v[46:47], v14, s[24:27], s68 offen\n\t"
      "v_fma_mix_f32 v148, v140, v125, v129 op_sel:[1,0,0] op_sel_hi:[1,0,0]\n\t"
      "v_fma_mix_f32 v149, v141, v127, v129 op_sel:[1,0,0] op_sel_hi:[1,0,0]\n\t"
      "v_cvt_pk_bf16_f32 v20, v144, v145\n\t"
      "v_cvt_pk_bf16_f32 v21, v146, v147\n\t"
      "v_fma_mix_f32 v150, v142, v125, v129 op_sel:[1,0,0] op_sel_hi:[1,0,0]\n\t"
      "v_fma_mix_f32 v151, v143, v127, v129 op_sel:[1,0,0] op_sel_hi:[1,0,0]\n\t"
      "v_cvt_pk_bf16_f32 v22, v148, v149\n\t"
      "v_cvt_pk_bf16_f32 v23, v150, v151\n\t"
      "s_waitcnt vmcnt(16)\n\t"
      "s_barrier\n\t"
      "ds_read_b128 a[0:3], v152 offset:0\n\t"
      "ds_read_b128 a[4:7], v152 offset:2048\n\t"
      "ds_read_b128 a[8:11], v152 offset:4096\n\t"
      "ds_read_b128 a[12:15], v152 offset:6144\n\t"
      "ds_read_b128 a[16:19], v152 offset:8192\n\t"
      "ds_read_b128 a[20:23], v152 offset:10240\n\t"
      "ds_read_b128 a[24:27], v152 offset:12288\n\t"
      "ds_read_b128 a[28:31], v152 offset:14336\n\t"
      "ds_read_b128 a[32:35], v152 offset:16384\n\t"
      "ds_read_b128 a[36:39], v152 offset:18432\n\t"
      "ds_read_b128 a[40:43], v152 offset:20480\n\t"
      "ds_read_b128 a[44:47], v152 offset:22528\n\t"
      "ds_read_b128 a[48:51], v152 offset:24576\n\t"
      "ds_read_b128 a[52:55], v152 offset:26624\n\t"
      "ds_read_b128 a[56:59], v152 offset:28672\n\t"
      "s_waitcnt lgkmcnt(0)\n\t"
      ".p2align 3\n\t"
      "v_mfma_f32_16x16x32_bf16 a[120:123], a[0:3], v[16:19], 0\n\t"
      "ds_read_b128 a[60:63], v153 offset:0\n\t"
      "v_lshrrev_b32_e64 v139, 8, v98\n\t"
      "v_mfma_f32_16x16x32_bf16 a[124:127], a[0:3], v[20:23], 0\n\t"
      "s_add_u32 m0, s19, 65536\n\t"
      "ds_read_b128 a[64:67], v153 offset:2048\n\t"
      "v_mfma_f32_16x16x32_bf16 a[128:131], a[4:7], v[16:19], 0\n\t"
      "buffer_load_dwordx4 v1, s[20:23], s42 offen offset:0 lds\n\t"
      "ds_read_b128 a[68:71], v153 offset:4096\n\t"
      "v_lshrrev_b32_e64 v143, 8, v99\n\t"
      "v_mfma_f32_16x16x32_bf16 a[132:135], a[4:7], v[20:23], 0\n\t"
      "buffer_load_dwordx2 v[104:105], v13, s[24:27], s58 offen\n\t"
      "v_and_b32_e64 v136, v11, v98\n\t"
      "v_mfma_f32_16x16x32_bf16 a[136:139], a[8:11], v[16:19], 0\n\t"
      "ds_read_b128 a[72:75], v153 offset:6144\n\t"
      "v_and_b32_e64 v137, v12, v98\n\t"
      "v_mfma_f32_16x16x32_bf16 a[140:143], a[8:11], v[20:23], 0\n\t"
      "ds_read_b128 a[76:79], v153 offset:8192\n\t"
      "v_and_b32 v140, v11, v99\n\t"
      "v_and_b32 v141, v12, v99\n\t"
      "v_mfma_f32_16x16x32_bf16 v[160:163], a[12:15], v[16:19], 0\n\t"
      "buffer_load_dwordx2 v[106:107], v14, s[24:27], s58 offen\n\t"
      "ds_read_b128 a[80:83], v153 offset:10240\n\t"
      "v_mfma_f32_16x16x32_bf16 v[164:167], a[12:15], v[20:23], 0\n\t"
      "buffer_load_dwordx4 v2, s[20:23], s42 offen offset:1024 lds\n\t"
      "v_and_b32_e64 v138, v11, v139\n\t"
      "v_mfma_f32_16x16x32_bf16 v[168:171], a[16:19], v[16:19], 0\n\t"
      "ds_read_b128 a[84:87], v153 offset:12288\n\t"
      "v_and_b32 v139, v12, v139\n\t"
      "v_and_b32 v142, v11, v143\n\t"
      "v_mfma_f32_16x16x32_bf16 v[172:175], a[16:19], v[20:23], 0\n\t"
      "buffer_load_dword v118, v15, s[28:31], s75 offen\n\t"
      "ds_read_b128 a[88:91], v153 offset:14336\n\t"
      "v_mfma_f32_16x16x32_bf16 v[176:179], a[20:23], v[16:19], 0\n\t"
      "ds_read_b128 a[92:95], v153 offset:16384\n\t"
      "v_and_b32_e64 v143, v12, v143\n\t"
      "v_mfma_f32_16x16x32_bf16 v[180:183], a[20:23], v[20:23], 0\n\t"
      "v_lshlrev_b32 v120, 16, v114\n\t"
      "v_and_b32 v121, v10, v114\n\t"
      "v_lshlrev_b32_e64 v122, 16, v115\n\t"
      "v_mfma_f32_16x16x32_bf16 v[184:187], a[24:27], v[16:19], 0\n\t"
      "buffer_load_dwordx4 v3, s[20:23], s42 offen offset:2048 lds\n\t"
      "ds_read_b128 a[96:99], v153 offset:18432\n\t"
      "v_mfma_f32_16x16x32_bf16 v[188:191], a[24:27], v[20:23], 0\n\t"
      "buffer_load_dword v119, v15, s[4:7], s75 offen\n\t"
      "ds_read_b128 a[100:103], v153 offset:20480\n\t"
      "v_mfma_f32_16x16x32_bf16 v[192:195], a[28:31], v[16:19], 0\n\t"
      "ds_read_b128 a[104:107], v153 offset:22528\n\t"
      "v_and_b32_e64 v123, v10, v115\n\t"
      "v_fma_mix_f32 v144, v136, v124, v128 op_sel_hi:[1,0,0]\n\t"
      "v_mfma_f32_16x16x32_bf16 v[196:199], a[28:31], v[20:23], 0\n\t"
      "buffer_load_dword v52, v15, s[28:31], s85 offen\n\t"
      "v_fma_mix_f32 v145, v137, v126, v128 op_sel_hi:[1,0,0]\n\t"
      "v_mfma_f32_16x16x32_bf16 v[200:203], a[32:35], v[16:19], 0\n\t"
      "ds_read_b128 a[108:111], v153 offset:24576\n\t"
      "v_fma_mix_f32 v146, v138, v124, v128 op_sel_hi:[1,0,0]\n\t"
      "v_mfma_f32_16x16x32_bf16 v[204:207], a[32:35], v[20:23], 0\n\t"
      "ds_read_b128 a[112:115], v153 offset:26624\n\t"
      "v_fma_mix_f32 v147, v139, v126, v128 op_sel_hi:[1,0,0]\n\t"
      "v_fma_mix_f32 v148, v136, v124, v128 op_sel:[1,0,0] op_sel_hi:[1,0,0]\n\t"
      "v_mfma_f32_16x16x32_bf16 v[208:211], a[36:39], v[16:19], 0\n\t"
      "buffer_load_dwordx4 v4, s[20:23], s42 offen offset:3072 lds\n\t"
      "ds_read_b128 a[116:119], v153 offset:28672\n\t"
      "v_mfma_f32_16x16x32_bf16 v[212:215], a[36:39], v[20:23], 0\n\t"
      "buffer_load_dword v53, v15, s[4:7], s85 offen\n\t"
      "v_fma_mix_f32 v149, v137, v126, v128 op_sel:[1,0,0] op_sel_hi:[1,0,0]\n\t"
      "v_mfma_f32_16x16x32_bf16 v[216:219], a[40:43], v[16:19], 0\n\t"
      "v_cvt_pk_bf16_f32 v24, v144, v145\n\t"
      "v_cvt_pk_bf16_f32 v25, v146, v147\n\t"
      "v_fma_mix_f32 v150, v138, v124, v128 op_sel:[1,0,0] op_sel_hi:[1,0,0]\n\t"
      "v_mfma_f32_16x16x32_bf16 v[220:223], a[40:43], v[20:23], 0\n\t"
      "buffer_load_dwordx2 v[54:55], v13, s[24:27], s69 offen\n\t"
      "v_fma_mix_f32 v151, v139, v126, v128 op_sel:[1,0,0] op_sel_hi:[1,0,0]\n\t"
      "v_mfma_f32_16x16x32_bf16 v[224:227], a[44:47], v[16:19], 0\n\t"
      "s_add_u32 m0, s19, 69632\n\t"
      "v_cvt_pk_bf16_f32 v26, v148, v149\n\t"
      "v_mfma_f32_16x16x32_bf16 v[228:231], a[44:47], v[20:23], 0\n\t"
      "buffer_load_dwordx4 v5, s[20:23], s42 offen offset:0 lds\n\t"
      "v_cvt_pk_bf16_f32 v27, v150, v151\n\t"
      "v_fma_mix_f32 v144, v140, v125, v129 op_sel_hi:[1,0,0]\n\t"
      "v_mfma_f32_16x16x32_bf16 v[232:235], a[48:51], v[16:19], 0\n\t"
      "buffer_load_dwordx2 v[62:63], v14, s[24:27], s69 offen\n\t"
      "v_fma_mix_f32 v145, v141, v127, v129 op_sel_hi:[1,0,0]\n\t"
      "v_mfma_f32_16x16x32_bf16 v[236:239], a[48:51], v[20:23], 0\n\t"
      "v_fma_mix_f32 v146, v142, v125, v129 op_sel_hi:[1,0,0]\n\t"
      "v_fma_mix_f32 v147, v143, v127, v129 op_sel_hi:[1,0,0]\n\t"
      "v_mfma_f32_16x16x32_bf16 v[240:243], a[52:55], v[16:19], 0\n\t"
      "v_fma_mix_f32 v148, v140, v125, v129 op_sel:[1,0,0] op_sel_hi:[1,0,0]\n\t"
      "v_fma_mix_f32 v149, v141, v127, v129 op_sel:[1,0,0] op_sel_hi:[1,0,0]\n\t"
      "v_cvt_pk_bf16_f32 v28, v144, v145\n\t"
      "v_mfma_f32_16x16x32_bf16 v[244:247], a[52:55], v[20:23], 0\n\t"
      "v_cvt_pk_bf16_f32 v29, v146, v147\n\t"
      "v_fma_mix_f32 v150, v142, v125, v129 op_sel:[1,0,0] op_sel_hi:[1,0,0]\n\t"
      "v_mfma_f32_16x16x32_bf16 v[248:251], a[56:59], v[16:19], 0\n\t"
      "v_fma_mix_f32 v151, v143, v127, v129 op_sel:[1,0,0] op_sel_hi:[1,0,0]\n\t"
      "v_cvt_pk_bf16_f32 v30, v148, v149\n\t"
      "v_cvt_pk_bf16_f32 v31, v150, v151\n\t"
      "v_mfma_f32_16x16x32_bf16 v[252:255], a[56:59], v[20:23], 0\n\t"
      "s_waitcnt vmcnt(13) lgkmcnt(0)\n\t"
      "s_barrier\n\t"
      "v_mfma_f32_16x16x32_bf16 a[120:123], a[60:63], v[24:27], a[120:123]\n\t"
      "ds_read_b128 a[0:3], v152 offset:32768\n\t"
      "v_lshlrev_b32 v144, 16, v36\n\t"
      "v_and_b32 v145, v10, v36\n\t"
      "v_mfma_f32_16x16x32_bf16 a[124:127], a[60:63], v[28:31], a[124:127]\n\t"
      "ds_read_b128 a[4:7], v152 offset:34816\n\t"
      "v_lshlrev_b32 v146, 16, v37\n\t"
      "v_and_b32 v147, v10, v37\n\t"
      "v_mul_f32 v130, 0x4b800000, v144\n\t"
      "v_mfma_f32_16x16x32_bf16 a[128:131], a[64:67], v[24:27], a[128:131]\n\t"
      "buffer_load_dwordx4 v6, s[20:23], s42 offen offset:1024 lds\n\t"
      "ds_read_b128 a[8:11], v152 offset:36864\n\t"
      "v_mul_f32 v132, 0x49800000, v144\n\t"
      "v_mul_f32_e64 v134, -v146, v144\n\t"
      "v_mfma_f32_16x16x32_bf16 a[132:135], a[64:67], v[28:31], a[132:135]\n\t"
      "buffer_load_dwordx2 v[108:109], v13, s[24:27], s59 offen\n\t"
      "v_mul_f32 v131, 0x4b800000, v145\n\t"
      "v_mul_f32 v133, 0x49800000, v145\n\t"
      "v_mfma_f32_16x16x32_bf16 a[136:139], a[68:71], v[24:27], a[136:139]\n\t"
      "ds_read_b128 a[12:15], v152 offset:38912\n\t"
      "v_mul_f32_e64 v135, -v147, v145\n\t"
      "v_lshrrev_b32 v139, 8, v38\n\t"
      "v_lshrrev_b32 v143, 8, v39\n\t"
      "v_mfma_f32_16x16x32_bf16 a[140:143], a[68:71], v[28:31], a[140:143]\n\t"
      "ds_read_b128 a[16:19], v152 offset:40960\n\t"
      "v_and_b32 v136, v11, v38\n\t"
      "v_and_b32 v137, v12, v38\n\t"
      "v_and_b32_e64 v140, v11, v39\n\t"
      "v_mfma_f32_16x16x32_bf16 v[160:163], a[72:75], v[24:27], v[160:163]\n\t"
      "ds_read_b128 a[20:23], v152 offset:43008\n\t"
      "v_and_b32 v141, v12, v39\n\t"
      "v_and_b32 v138, v11, v139\n\t"
      "v_mfma_f32_16x16x32_bf16 v[164:167], a[72:75], v[28:31], v[164:167]\n\t"
      "buffer_load_dwordx4 v7, s[20:23], s42 offen offset:2048 lds\n\t"
      "v_and_b32 v139, v12, v139\n\t"
      "v_and_b32 v142, v11, v143\n\t"
      "v_and_b32_e64 v143, v12, v143\n\t"
      "v_mfma_f32_16x16x32_bf16 v[168:171], a[76:79], v[24:27], v[168:171]\n\t"
      "ds_read_b128 a[24:27], v152 offset:45056\n\t"
      "v_fma_mix_f32 v144, v136, v130, v134 op_sel_hi:[1,0,0]\n\t"
      "v_fma_mix_f32 v145, v137, v132, v134 op_sel_hi:[1,0,0]\n\t"
      "v_fma_mix_f32 v146, v138, v130, v134 op_sel_hi:[1,0,0]\n\t"
      "v_mfma_f32_16x16x32_bf16 v[172:175], a[76:79], v[28:31], v[172:175]\n\t"
      "ds_read_b128 a[28:31], v152 offset:47104\n\t"
      "v_fma_mix_f32 v147, v139, v132, v134 op_sel_hi:[1,0,0]\n\t"
      "v_fma_mix_f32 v148, v136, v130, v134 op_sel:[1,0,0] op_sel_hi:[1,0,0]\n\t"
      "v_mfma_f32_16x16x32_bf16 v[176:179], a[80:83], v[24:27], v[176:179]\n\t"
      "ds_read_b128 a[32:35], v152 offset:49152\n\t"
      "v_fma_mix_f32 v149, v137, v132, v134 op_sel:[1,0,0] op_sel_hi:[1,0,0]\n\t"
      "v_cvt_pk_bf16_f32 v32, v144, v145\n\t"
      "v_cvt_pk_bf16_f32 v33, v146, v147\n\t"
      "v_mfma_f32_16x16x32_bf16 v[180:183], a[80:83], v[28:31], v[180:183]\n\t"
      "buffer_load_dword v112, v15, s[28:31], s76 offen\n\t"
      "v_fma_mix_f32 v150, v138, v130, v134 op_sel:[1,0,0] op_sel_hi:[1,0,0]\n\t"
      "v_fma_mix_f32 v151, v139, v132, v134 op_sel:[1,0,0] op_sel_hi:[1,0,0]\n\t"
      "v_cvt_pk_bf16_f32 v34, v148, v149\n\t"
      "v_mfma_f32_16x16x32_bf16 v[184:187], a[84:87], v[24:27], v[184:187]\n\t"
      "buffer_load_dwordx4 v8, s[20:23], s42 offen offset:3072 lds\n\t"
      "ds_read_b128 a[36:39], v152 offset:51200\n\t"
      "v_cvt_pk_bf16_f32 v35, v150, v151\n\t"
      "v_mfma_f32_16x16x32_bf16 v[188:191], a[84:87], v[28:31], v[188:191]\n\t"
      "ds_read_b128 a[40:43], v152 offset:53248\n\t"
      "v_fma_mix_f32 v144, v140, v131, v135 op_sel_hi:[1,0,0]\n\t"
      "v_fma_mix_f32 v145, v141, v133, v135 op_sel_hi:[1,0,0]\n\t"
      "v_fma_mix_f32 v146, v142, v131, v135 op_sel_hi:[1,0,0]\n\t"
      "v_mfma_f32_16x16x32_bf16 v[192:195], a[88:91], v[24:27], v[192:195]\n\t"
      "ds_read_b128 a[44:47], v152 offset:55296\n\t"
      "v_fma_mix_f32 v147, v143, v133, v135 op_sel_hi:[1,0,0]\n\t"
      "v_fma_mix_f32 v148, v140, v131, v135 op_sel:[1,0,0] op_sel_hi:[1,0,0]\n\t"
      "v_fma_mix_f32 v149, v141, v133, v135 op_sel:[1,0,0] op_sel_hi:[1,0,0]\n\t"
      "v_mfma_f32_16x16x32_bf16 v[196:199], a[88:91], v[28:31], v[196:199]\n\t"
      "v_cvt_pk_bf16_f32 v36, v144, v145\n\t"
      "v_cvt_pk_bf16_f32 v37, v146, v147\n\t"
      "v_fma_mix_f32 v150, v142, v131, v135 op_sel:[1,0,0] op_sel_hi:[1,0,0]\n\t"
      "v_fma_mix_f32 v151, v143, v133, v135 op_sel:[1,0,0] op_sel_hi:[1,0,0]\n\t"
      "v_mfma_f32_16x16x32_bf16 v[200:203], a[92:95], v[24:27], v[200:203]\n\t"
      "ds_read_b128 a[48:51], v152 offset:57344\n\t"
      "v_cvt_pk_bf16_f32 v38, v148, v149\n\t"
      "v_cvt_pk_bf16_f32 v39, v150, v151\n\t"
      "v_mfma_f32_16x16x32_bf16 v[204:207], a[92:95], v[28:31], v[204:207]\n\t"
      "s_add_u32 m0, s19, 98304\n\t"
      "ds_read_b128 a[52:55], v152 offset:59392\n\t"
      "v_lshrrev_b32 v139, 8, v100\n\t"
      "v_lshrrev_b32 v143, 8, v101\n\t"
      "v_mfma_f32_16x16x32_bf16 v[208:211], a[96:99], v[24:27], v[208:211]\n\t"
      "buffer_load_dwordx4 v1, s[20:23], s43 offen offset:0 lds\n\t"
      "ds_read_b128 a[56:59], v152 offset:61440\n\t"
      "v_and_b32 v136, v11, v100\n\t"
      "v_and_b32 v137, v12, v100\n\t"
      "v_mfma_f32_16x16x32_bf16 v[212:215], a[96:99], v[28:31], v[212:215]\n\t"
      "buffer_load_dword v113, v15, s[4:7], s76 offen\n\t"
      "v_and_b32 v140, v11, v101\n\t"
      "v_and_b32 v141, v12, v101\n\t"
      "v_mfma_f32_16x16x32_bf16 v[216:219], a[100:103], v[24:27], v[216:219]\n\t"
      "v_and_b32 v138, v11, v139\n\t"
      "v_and_b32 v139, v12, v139\n\t"
      "v_and_b32 v142, v11, v143\n\t"
      "v_and_b32 v143, v12, v143\n\t"
      "v_mfma_f32_16x16x32_bf16 v[220:223], a[100:103], v[28:31], v[220:223]\n\t"
      "v_mul_f32 v124, 0x4b800000, v120\n\t"
      "v_mul_f32 v126, 0x49800000, v120\n\t"
      "v_mul_f32_e64 v128, -v122, v120\n\t"
      "v_mul_f32 v125, 0x4b800000, v121\n\t"
      "v_mfma_f32_16x16x32_bf16 v[224:227], a[104:107], v[24:27], v[224:227]\n\t"
      "v_mul_f32 v127, 0x49800000, v121\n\t"
      "v_mul_f32_e64 v129, -v123, v121\n\t"
      "v_fma_mix_f32 v144, v136, v124, v128 op_sel_hi:[1,0,0]\n\t"
      "v_mfma_f32_16x16x32_bf16 v[228:231], a[104:107], v[28:31], v[228:231]\n\t"
      "buffer_load_dwordx4 v2, s[20:23], s43 offen offset:1024 lds\n\t"
      "v_fma_mix_f32 v145, v137, v126, v128 op_sel_hi:[1,0,0]\n\t"
      "v_fma_mix_f32 v146, v138, v124, v128 op_sel_hi:[1,0,0]\n\t"
      "v_fma_mix_f32 v147, v139, v126, v128 op_sel_hi:[1,0,0]\n\t"
      "v_mfma_f32_16x16x32_bf16 v[232:235], a[108:111], v[24:27], v[232:235]\n\t"
      "v_fma_mix_f32 v148, v136, v124, v128 op_sel:[1,0,0] op_sel_hi:[1,0,0]\n\t"
      "v_fma_mix_f32 v149, v137, v126, v128 op_sel:[1,0,0] op_sel_hi:[1,0,0]\n\t"
      "v_cvt_pk_bf16_f32 v16, v144, v145\n\t"
      "v_cvt_pk_bf16_f32 v17, v146, v147\n\t"
      "v_mfma_f32_16x16x32_bf16 v[236:239], a[108:111], v[28:31], v[236:239]\n\t"
      "v_fma_mix_f32 v150, v138, v124, v128 op_sel:[1,0,0] op_sel_hi:[1,0,0]\n\t"
      "v_fma_mix_f32 v151, v139, v126, v128 op_sel:[1,0,0] op_sel_hi:[1,0,0]\n\t"
      "v_cvt_pk_bf16_f32 v18, v148, v149\n\t"
      "v_mfma_f32_16x16x32_bf16 v[240:243], a[112:115], v[24:27], v[240:243]\n\t"
      "v_cvt_pk_bf16_f32 v19, v150, v151\n\t"
      "v_fma_mix_f32 v144, v140, v125, v129 op_sel_hi:[1,0,0]\n\t"
      "v_fma_mix_f32 v145, v141, v127, v129 op_sel_hi:[1,0,0]\n\t"
      "v_fma_mix_f32 v146, v142, v125, v129 op_sel_hi:[1,0,0]\n\t"
      "v_mfma_f32_16x16x32_bf16 v[244:247], a[112:115], v[28:31], v[244:247]\n\t"
      "v_fma_mix_f32 v147, v143, v127, v129 op_sel_hi:[1,0,0]\n\t"
      "v_fma_mix_f32 v148, v140, v125, v129 op_sel:[1,0,0] op_sel_hi:[1,0,0]\n\t"
      "v_fma_mix_f32 v149, v141, v127, v129 op_sel:[1,0,0] op_sel_hi:[1,0,0]\n\t"
      "v_cvt_pk_bf16_f32 v20, v144, v145\n\t"
      "v_mfma_f32_16x16x32_bf16 v[248:251], a[116:119], v[24:27], v[248:251]\n\t"
      "v_cvt_pk_bf16_f32 v21, v146, v147\n\t"
      "v_fma_mix_f32 v150, v142, v125, v129 op_sel:[1,0,0] op_sel_hi:[1,0,0]\n\t"
      "v_fma_mix_f32 v151, v143, v127, v129 op_sel:[1,0,0] op_sel_hi:[1,0,0]\n\t"
      "v_cvt_pk_bf16_f32 v22, v148, v149\n\t"
      "v_mfma_f32_16x16x32_bf16 v[252:255], a[116:119], v[28:31], v[252:255]\n\t"
      "v_cvt_pk_bf16_f32 v23, v150, v151\n\t"
      "s_waitcnt lgkmcnt(0)\n\t"
      "s_nop 0\n\t"
      "v_mfma_f32_16x16x32_bf16 a[120:123], a[0:3], v[16:19], a[120:123]\n\t"
      "ds_read_b128 a[60:63], v153 offset:32768\n\t"
      "v_lshrrev_b32 v139, 8, v46\n\t"
      "v_lshrrev_b32 v143, 8, v47\n\t"
      "v_mfma_f32_16x16x32_bf16 a[124:127], a[0:3], v[20:23], a[124:127]\n\t"
      "ds_read_b128 a[64:67], v153 offset:34816\n\t"
      "v_and_b32 v136, v11, v46\n\t"
      "v_and_b32 v137, v12, v46\n\t"
      "v_mfma_f32_16x16x32_bf16 a[128:131], a[4:7], v[16:19], a[128:131]\n\t"
      "buffer_load_dwordx4 v3, s[20:23], s43 offen offset:2048 lds\n\t"
      "ds_read_b128 a[68:71], v153 offset:36864\n\t"
      "v_and_b32 v140, v11, v47\n\t"
      "v_and_b32 v141, v12, v47\n\t"
      "v_mfma_f32_16x16x32_bf16 a[132:135], a[4:7], v[20:23], a[132:135]\n\t"
      "buffer_load_dwordx2 v[110:111], v14, s[24:27], s59 offen\n\t"
      "v_and_b32 v138, v11, v139\n\t"
      "v_and_b32 v139, v12, v139\n\t"
      "v_mfma_f32_16x16x32_bf16 a[136:139], a[8:11], v[16:19], a[136:139]\n\t"
      "ds_read_b128 a[72:75], v153 offset:38912\n\t"
      "v_and_b32 v142, v11, v143\n\t"
      "v_and_b32 v143, v12, v143\n\t"
      "v_mfma_f32_16x16x32_bf16 a[140:143], a[8:11], v[20:23], a[140:143]\n\t"
      "ds_read_b128 a[76:79], v153 offset:40960\n\t"
      "v_fma_mix_f32 v144, v136, v130, v134 op_sel_hi:[1,0,0]\n\t"
      "v_fma_mix_f32 v145, v137, v132, v134 op_sel_hi:[1,0,0]\n\t"
      "v_fma_mix_f32 v146, v138, v130, v134 op_sel_hi:[1,0,0]\n\t"
      "v_mfma_f32_16x16x32_bf16 v[160:163], a[12:15], v[16:19], v[160:163]\n\t"
      "ds_read_b128 a[80:83], v153 offset:43008\n\t"
      "v_fma_mix_f32 v147, v139, v132, v134 op_sel_hi:[1,0,0]\n\t"
      "v_fma_mix_f32 v148, v136, v130, v134 op_sel:[1,0,0] op_sel_hi:[1,0,0]\n\t"
      "v_mfma_f32_16x16x32_bf16 v[164:167], a[12:15], v[20:23], v[164:167]\n\t"
      "buffer_load_dwordx4 v4, s[20:23], s43 offen offset:3072 lds\n\t"
      "v_fma_mix_f32 v149, v137, v132, v134 op_sel:[1,0,0] op_sel_hi:[1,0,0]\n\t"
      "v_cvt_pk_bf16_f32 v40, v144, v145\n\t"
      "v_cvt_pk_bf16_f32 v41, v146, v147\n\t"
      "v_mfma_f32_16x16x32_bf16 v[168:171], a[16:19], v[16:19], v[168:171]\n\t"
      "buffer_load_dword v68, v15, s[28:31], s86 offen\n\t"
      "ds_read_b128 a[84:87], v153 offset:45056\n\t"
      "v_fma_mix_f32 v150, v138, v130, v134 op_sel:[1,0,0] op_sel_hi:[1,0,0]\n\t"
      "v_mfma_f32_16x16x32_bf16 v[172:175], a[16:19], v[20:23], v[172:175]\n\t"
      "ds_read_b128 a[88:91], v153 offset:47104\n\t"
      "v_fma_mix_f32 v151, v139, v132, v134 op_sel:[1,0,0] op_sel_hi:[1,0,0]\n\t"
      "v_cvt_pk_bf16_f32 v42, v148, v149\n\t"
      "v_mfma_f32_16x16x32_bf16 v[176:179], a[20:23], v[16:19], v[176:179]\n\t"
      "ds_read_b128 a[92:95], v153 offset:49152\n\t"
      "v_cvt_pk_bf16_f32 v43, v150, v151\n\t"
      "v_fma_mix_f32 v144, v140, v131, v135 op_sel_hi:[1,0,0]\n\t"
      "v_fma_mix_f32 v145, v141, v133, v135 op_sel_hi:[1,0,0]\n\t"
      "v_mfma_f32_16x16x32_bf16 v[180:183], a[20:23], v[20:23], v[180:183]\n\t"
      "s_add_u32 m0, s19, 102400\n\t"
      "v_fma_mix_f32 v146, v142, v131, v135 op_sel_hi:[1,0,0]\n\t"
      "v_fma_mix_f32 v147, v143, v133, v135 op_sel_hi:[1,0,0]\n\t"
      "v_mfma_f32_16x16x32_bf16 v[184:187], a[24:27], v[16:19], v[184:187]\n\t"
      "buffer_load_dwordx4 v5, s[20:23], s43 offen offset:0 lds\n\t"
      "ds_read_b128 a[96:99], v153 offset:51200\n\t"
      "v_fma_mix_f32 v148, v140, v131, v135 op_sel:[1,0,0] op_sel_hi:[1,0,0]\n\t"
      "v_mfma_f32_16x16x32_bf16 v[188:191], a[24:27], v[20:23], v[188:191]\n\t"
      "buffer_load_dword v69, v15, s[4:7], s86 offen\n\t"
      "ds_read_b128 a[100:103], v153 offset:53248\n\t"
      "v_fma_mix_f32 v149, v141, v133, v135 op_sel:[1,0,0] op_sel_hi:[1,0,0]\n\t"
      "v_cvt_pk_bf16_f32 v44, v144, v145\n\t"
      "v_mfma_f32_16x16x32_bf16 v[192:195], a[28:31], v[16:19], v[192:195]\n\t"
      "ds_read_b128 a[104:107], v153 offset:55296\n\t"
      "v_cvt_pk_bf16_f32 v45, v146, v147\n\t"
      "v_fma_mix_f32 v150, v142, v131, v135 op_sel:[1,0,0] op_sel_hi:[1,0,0]\n\t"
      "v_mfma_f32_16x16x32_bf16 v[196:199], a[28:31], v[20:23], v[196:199]\n\t"
      "v_fma_mix_f32 v151, v143, v133, v135 op_sel:[1,0,0] op_sel_hi:[1,0,0]\n\t"
      "v_cvt_pk_bf16_f32 v46, v148, v149\n\t"
      "v_cvt_pk_bf16_f32 v47, v150, v151\n\t"
      "v_lshrrev_b32_e64 v139, 8, v102\n\t"
      "v_mfma_f32_16x16x32_bf16 v[200:203], a[32:35], v[16:19], v[200:203]\n\t"
      "ds_read_b128 a[108:111], v153 offset:57344\n\t"
      "v_lshrrev_b32 v143, 8, v103\n\t"
      "v_and_b32 v136, v11, v102\n\t"
      "v_mfma_f32_16x16x32_bf16 v[204:207], a[32:35], v[20:23], v[204:207]\n\t"
      "ds_read_b128 a[112:115], v153 offset:59392\n\t"
      "v_and_b32 v137, v12, v102\n\t"
      "v_and_b32 v140, v11, v103\n\t"
      "v_mfma_f32_16x16x32_bf16 v[208:211], a[36:39], v[16:19], v[208:211]\n\t"
      "buffer_load_dwordx4 v6, s[20:23], s43 offen offset:1024 lds\n\t"
      "ds_read_b128 a[116:119], v153 offset:61440\n\t"
      "v_and_b32 v141, v12, v103\n\t"
      "v_and_b32 v138, v11, v139\n\t"
      "v_mfma_f32_16x16x32_bf16 v[212:215], a[36:39], v[20:23], v[212:215]\n\t"
      "buffer_load_dwordx2 v[70:71], v13, s[24:27], s70 offen\n\t"
      "v_and_b32 v139, v12, v139\n\t"
      "v_and_b32 v142, v11, v143\n\t"
      "v_mfma_f32_16x16x32_bf16 v[216:219], a[40:43], v[16:19], v[216:219]\n\t"
      "v_and_b32 v143, v12, v143\n\t"
      "v_lshlrev_b32 v120, 16, v116\n\t"
      "v_and_b32_e64 v121, v10, v116\n\t"
      "v_mfma_f32_16x16x32_bf16 v[220:223], a[40:43], v[20:23], v[220:223]\n\t"
      "v_lshlrev_b32 v122, 16, v117\n\t"
      "v_and_b32 v123, v10, v117\n\t"
      "v_fma_mix_f32 v144, v136, v124, v128 op_sel_hi:[1,0,0]\n\t"
      "v_fma_mix_f32 v145, v137, v126, v128 op_sel_hi:[1,0,0]\n\t"
      "v_mfma_f32_16x16x32_bf16 v[224:227], a[44:47], v[16:19], v[224:227]\n\t"
      "v_fma_mix_f32 v146, v138, v124, v128 op_sel_hi:[1,0,0]\n\t"
      "v_fma_mix_f32 v147, v139, v126, v128 op_sel_hi:[1,0,0]\n\t"
      "v_fma_mix_f32 v148, v136, v124, v128 op_sel:[1,0,0] op_sel_hi:[1,0,0]\n\t"
      "v_mfma_f32_16x16x32_bf16 v[228:231], a[44:47], v[20:23], v[228:231]\n\t"
      "buffer_load_dwordx4 v7, s[20:23], s43 offen offset:2048 lds\n\t"
      "v_fma_mix_f32 v149, v137, v126, v128 op_sel:[1,0,0] op_sel_hi:[1,0,0]\n\t"
      "v_cvt_pk_bf16_f32 v24, v144, v145\n\t"
      "v_cvt_pk_bf16_f32 v25, v146, v147\n\t"
      "v_mfma_f32_16x16x32_bf16 v[232:235], a[48:51], v[16:19], v[232:235]\n\t"
      "buffer_load_dwordx2 v[78:79], v14, s[24:27], s70 offen\n\t"
      "v_fma_mix_f32 v150, v138, v124, v128 op_sel:[1,0,0] op_sel_hi:[1,0,0]\n\t"
      "v_fma_mix_f32 v151, v139, v126, v128 op_sel:[1,0,0] op_sel_hi:[1,0,0]\n\t"
      "v_mfma_f32_16x16x32_bf16 v[236:239], a[48:51], v[20:23], v[236:239]\n\t"
      "v_cvt_pk_bf16_f32 v26, v148, v149\n\t"
      "v_cvt_pk_bf16_f32 v27, v150, v151\n\t"
      "v_fma_mix_f32 v144, v140, v125, v129 op_sel_hi:[1,0,0]\n\t"
      "v_mfma_f32_16x16x32_bf16 v[240:243], a[52:55], v[16:19], v[240:243]\n\t"
      "v_fma_mix_f32 v145, v141, v127, v129 op_sel_hi:[1,0,0]\n\t"
      "v_fma_mix_f32 v146, v142, v125, v129 op_sel_hi:[1,0,0]\n\t"
      "v_fma_mix_f32 v147, v143, v127, v129 op_sel_hi:[1,0,0]\n\t"
      "v_fma_mix_f32 v148, v140, v125, v129 op_sel:[1,0,0] op_sel_hi:[1,0,0]\n\t"
      "v_mfma_f32_16x16x32_bf16 v[244:247], a[52:55], v[20:23], v[244:247]\n\t"
      "v_fma_mix_f32 v149, v141, v127, v129 op_sel:[1,0,0] op_sel_hi:[1,0,0]\n\t"
      "v_cvt_pk_bf16_f32 v28, v144, v145\n\t"
      "v_cvt_pk_bf16_f32 v29, v146, v147\n\t"
      "v_mfma_f32_16x16x32_bf16 v[248:251], a[56:59], v[16:19], v[248:251]\n\t"
      "v_fma_mix_f32 v150, v142, v125, v129 op_sel:[1,0,0] op_sel_hi:[1,0,0]\n\t"
      "v_fma_mix_f32 v151, v143, v127, v129 op_sel:[1,0,0] op_sel_hi:[1,0,0]\n\t"
      "v_cvt_pk_bf16_f32 v30, v148, v149\n\t"
      "v_cvt_pk_bf16_f32 v31, v150, v151\n\t"
      "v_mfma_f32_16x16x32_bf16 v[252:255], a[56:59], v[20:23], v[252:255]\n\t"
      "s_waitcnt vmcnt(13) lgkmcnt(0)\n\t"
      "s_barrier\n\t"
      "v_mfma_f32_16x16x32_bf16 a[120:123], a[60:63], v[24:27], a[120:123]\n\t"
      "ds_read_b128 a[0:3], v154 offset:0\n\t"
      "v_lshlrev_b32 v144, 16, v52\n\t"
      "v_and_b32 v145, v10, v52\n\t"
      "v_mfma_f32_16x16x32_bf16 a[124:127], a[60:63], v[28:31], a[124:127]\n\t"
      "ds_read_b128 a[4:7], v154 offset:2048\n\t"
      "v_lshlrev_b32 v146, 16, v53\n\t"
      "v_and_b32 v147, v10, v53\n\t"
      "v_mul_f32 v130, 0x4b800000, v144\n\t"
      "v_mfma_f32_16x16x32_bf16 a[128:131], a[64:67], v[24:27], a[128:131]\n\t"
      "buffer_load_dwordx4 v8, s[20:23], s43 offen offset:3072 lds\n\t"
      "ds_read_b128 a[8:11], v154 offset:4096\n\t"
      "v_mul_f32 v132, 0x49800000, v144\n\t"
      "v_mul_f32_e64 v134, -v146, v144\n\t"
      "v_mfma_f32_16x16x32_bf16 a[132:135], a[64:67], v[28:31], a[132:135]\n\t"
      "buffer_load_dwordx2 v[96:97], v13, s[24:27], s60 offen\n\t"
      "v_mul_f32 v131, 0x4b800000, v145\n\t"
      "v_mul_f32 v133, 0x49800000, v145\n\t"
      "v_mfma_f32_16x16x32_bf16 a[136:139], a[68:71], v[24:27], a[136:139]\n\t"
      "ds_read_b128 a[12:15], v154 offset:6144\n\t"
      "v_mul_f32_e64 v135, -v147, v145\n\t"
      "v_lshrrev_b32 v139, 8, v54\n\t"
      "v_lshrrev_b32 v143, 8, v55\n\t"
      "v_mfma_f32_16x16x32_bf16 a[140:143], a[68:71], v[28:31], a[140:143]\n\t"
      "ds_read_b128 a[16:19], v154 offset:8192\n\t"
      "v_and_b32 v136, v11, v54\n\t"
      "v_and_b32 v137, v12, v54\n\t"
      "v_and_b32_e64 v140, v11, v55\n\t"
      "v_mfma_f32_16x16x32_bf16 v[160:163], a[72:75], v[24:27], v[160:163]\n\t"
      "s_add_u32 m0, s19, 131072\n\t"
      "ds_read_b128 a[20:23], v154 offset:10240\n\t"
      "v_and_b32_e64 v141, v12, v55\n\t"
      "v_mfma_f32_16x16x32_bf16 v[164:167], a[72:75], v[28:31], v[164:167]\n\t"
      "buffer_load_dwordx4 v1, s[20:23], s44 offen offset:0 lds\n\t"
      "v_and_b32 v138, v11, v139\n\t"
      "v_and_b32 v139, v12, v139\n\t"
      "v_and_b32_e64 v142, v11, v143\n\t"
      "v_mfma_f32_16x16x32_bf16 v[168:171], a[76:79], v[24:27], v[168:171]\n\t"
      "ds_read_b128 a[24:27], v154 offset:12288\n\t"
      "v_and_b32_e64 v143, v12, v143\n\t"
      "v_fma_mix_f32 v144, v136, v130, v134 op_sel_hi:[1,0,0]\n\t"
      "v_fma_mix_f32 v145, v137, v132, v134 op_sel_hi:[1,0,0]\n\t"
      "v_mfma_f32_16x16x32_bf16 v[172:175], a[76:79], v[28:31], v[172:175]\n\t"
      "ds_read_b128 a[28:31], v154 offset:14336\n\t"
      "v_fma_mix_f32 v146, v138, v130, v134 op_sel_hi:[1,0,0]\n\t"
      "v_fma_mix_f32 v147, v139, v132, v134 op_sel_hi:[1,0,0]\n\t"
      "v_mfma_f32_16x16x32_bf16 v[176:179], a[80:83], v[24:27], v[176:179]\n\t"
      "ds_read_b128 a[32:35], v154 offset:16384\n\t"
      "v_fma_mix_f32 v148, v136, v130, v134 op_sel:[1,0,0] op_sel_hi:[1,0,0]\n\t"
      "v_fma_mix_f32 v149, v137, v132, v134 op_sel:[1,0,0] op_sel_hi:[1,0,0]\n\t"
      "v_cvt_pk_bf16_f32 v48, v144, v145\n\t"
      "v_mfma_f32_16x16x32_bf16 v[180:183], a[80:83], v[28:31], v[180:183]\n\t"
      "buffer_load_dword v114, v15, s[28:31], s77 offen\n\t"
      "v_cvt_pk_bf16_f32 v49, v146, v147\n\t"
      "v_fma_mix_f32 v150, v138, v130, v134 op_sel:[1,0,0] op_sel_hi:[1,0,0]\n\t"
      "v_fma_mix_f32 v151, v139, v132, v134 op_sel:[1,0,0] op_sel_hi:[1,0,0]\n\t"
      "v_mfma_f32_16x16x32_bf16 v[184:187], a[84:87], v[24:27], v[184:187]\n\t"
      "buffer_load_dwordx4 v2, s[20:23], s44 offen offset:1024 lds\n\t"
      "ds_read_b128 a[36:39], v154 offset:18432\n\t"
      "v_cvt_pk_bf16_f32 v50, v148, v149\n\t"
      "v_mfma_f32_16x16x32_bf16 v[188:191], a[84:87], v[28:31], v[188:191]\n\t"
      "ds_read_b128 a[40:43], v154 offset:20480\n\t"
      "v_cvt_pk_bf16_f32 v51, v150, v151\n\t"
      "v_fma_mix_f32 v144, v140, v131, v135 op_sel_hi:[1,0,0]\n\t"
      "v_fma_mix_f32 v145, v141, v133, v135 op_sel_hi:[1,0,0]\n\t"
      "v_mfma_f32_16x16x32_bf16 v[192:195], a[88:91], v[24:27], v[192:195]\n\t"
      "ds_read_b128 a[44:47], v154 offset:22528\n\t"
      "v_fma_mix_f32 v146, v142, v131, v135 op_sel_hi:[1,0,0]\n\t"
      "v_fma_mix_f32 v147, v143, v133, v135 op_sel_hi:[1,0,0]\n\t"
      "v_fma_mix_f32 v148, v140, v131, v135 op_sel:[1,0,0] op_sel_hi:[1,0,0]\n\t"
      "v_mfma_f32_16x16x32_bf16 v[196:199], a[88:91], v[28:31], v[196:199]\n\t"
      "v_fma_mix_f32 v149, v141, v133, v135 op_sel:[1,0,0] op_sel_hi:[1,0,0]\n\t"
      "v_cvt_pk_bf16_f32 v52, v144, v145\n\t"
      "v_cvt_pk_bf16_f32 v53, v146, v147\n\t"
      "v_fma_mix_f32 v150, v142, v131, v135 op_sel:[1,0,0] op_sel_hi:[1,0,0]\n\t"
      "v_mfma_f32_16x16x32_bf16 v[200:203], a[92:95], v[24:27], v[200:203]\n\t"
      "ds_read_b128 a[48:51], v154 offset:24576\n\t"
      "v_fma_mix_f32 v151, v143, v133, v135 op_sel:[1,0,0] op_sel_hi:[1,0,0]\n\t"
      "v_cvt_pk_bf16_f32 v54, v148, v149\n\t"
      "v_mfma_f32_16x16x32_bf16 v[204:207], a[92:95], v[28:31], v[204:207]\n\t"
      "ds_read_b128 a[52:55], v154 offset:26624\n\t"
      "v_cvt_pk_bf16_f32 v55, v150, v151\n\t"
      "v_lshrrev_b32 v139, 8, v104\n\t"
      "v_lshrrev_b32 v143, 8, v105\n\t"
      "v_mfma_f32_16x16x32_bf16 v[208:211], a[96:99], v[24:27], v[208:211]\n\t"
      "buffer_load_dwordx4 v3, s[20:23], s44 offen offset:2048 lds\n\t"
      "ds_read_b128 a[56:59], v154 offset:28672\n\t"
      "v_and_b32 v136, v11, v104\n\t"
      "v_and_b32 v137, v12, v104\n\t"
      "v_mfma_f32_16x16x32_bf16 v[212:215], a[96:99], v[28:31], v[212:215]\n\t"
      "buffer_load_dword v115, v15, s[4:7], s77 offen\n\t"
      "v_and_b32 v140, v11, v105\n\t"
      "v_and_b32 v141, v12, v105\n\t"
      "v_mfma_f32_16x16x32_bf16 v[216:219], a[100:103], v[24:27], v[216:219]\n\t"
      "v_and_b32 v138, v11, v139\n\t"
      "v_and_b32 v139, v12, v139\n\t"
      "v_and_b32 v142, v11, v143\n\t"
      "v_and_b32 v143, v12, v143\n\t"
      "v_mfma_f32_16x16x32_bf16 v[220:223], a[100:103], v[28:31], v[220:223]\n\t"
      "v_mul_f32 v124, 0x4b800000, v120\n\t"
      "v_mul_f32 v126, 0x49800000, v120\n\t"
      "v_mul_f32_e64 v128, -v122, v120\n\t"
      "v_mul_f32 v125, 0x4b800000, v121\n\t"
      "v_mfma_f32_16x16x32_bf16 v[224:227], a[104:107], v[24:27], v[224:227]\n\t"
      "v_mul_f32 v127, 0x49800000, v121\n\t"
      "v_mul_f32_e64 v129, -v123, v121\n\t"
      "v_fma_mix_f32 v144, v136, v124, v128 op_sel_hi:[1,0,0]\n\t"
      "v_mfma_f32_16x16x32_bf16 v[228:231], a[104:107], v[28:31], v[228:231]\n\t"
      "buffer_load_dwordx4 v4, s[20:23], s44 offen offset:3072 lds\n\t"
      "v_fma_mix_f32 v145, v137, v126, v128 op_sel_hi:[1,0,0]\n\t"
      "v_fma_mix_f32 v146, v138, v124, v128 op_sel_hi:[1,0,0]\n\t"
      "v_fma_mix_f32 v147, v139, v126, v128 op_sel_hi:[1,0,0]\n\t"
      "v_mfma_f32_16x16x32_bf16 v[232:235], a[108:111], v[24:27], v[232:235]\n\t"
      "v_fma_mix_f32 v148, v136, v124, v128 op_sel:[1,0,0] op_sel_hi:[1,0,0]\n\t"
      "v_fma_mix_f32 v149, v137, v126, v128 op_sel:[1,0,0] op_sel_hi:[1,0,0]\n\t"
      "v_cvt_pk_bf16_f32 v16, v144, v145\n\t"
      "v_cvt_pk_bf16_f32 v17, v146, v147\n\t"
      "v_mfma_f32_16x16x32_bf16 v[236:239], a[108:111], v[28:31], v[236:239]\n\t"
      "v_fma_mix_f32 v150, v138, v124, v128 op_sel:[1,0,0] op_sel_hi:[1,0,0]\n\t"
      "v_fma_mix_f32 v151, v139, v126, v128 op_sel:[1,0,0] op_sel_hi:[1,0,0]\n\t"
      "v_cvt_pk_bf16_f32 v18, v148, v149\n\t"
      "v_mfma_f32_16x16x32_bf16 v[240:243], a[112:115], v[24:27], v[240:243]\n\t"
      "v_cvt_pk_bf16_f32 v19, v150, v151\n\t"
      "v_fma_mix_f32 v144, v140, v125, v129 op_sel_hi:[1,0,0]\n\t"
      "v_fma_mix_f32 v145, v141, v127, v129 op_sel_hi:[1,0,0]\n\t"
      "v_fma_mix_f32 v146, v142, v125, v129 op_sel_hi:[1,0,0]\n\t"
      "v_mfma_f32_16x16x32_bf16 v[244:247], a[112:115], v[28:31], v[244:247]\n\t"
      "v_fma_mix_f32 v147, v143, v127, v129 op_sel_hi:[1,0,0]\n\t"
      "v_fma_mix_f32 v148, v140, v125, v129 op_sel:[1,0,0] op_sel_hi:[1,0,0]\n\t"
      "v_fma_mix_f32 v149, v141, v127, v129 op_sel:[1,0,0] op_sel_hi:[1,0,0]\n\t"
      "v_cvt_pk_bf16_f32 v20, v144, v145\n\t"
      "v_mfma_f32_16x16x32_bf16 v[248:251], a[116:119], v[24:27], v[248:251]\n\t"
      "v_cvt_pk_bf16_f32 v21, v146, v147\n\t"
      "v_fma_mix_f32 v150, v142, v125, v129 op_sel:[1,0,0] op_sel_hi:[1,0,0]\n\t"
      "v_fma_mix_f32 v151, v143, v127, v129 op_sel:[1,0,0] op_sel_hi:[1,0,0]\n\t"
      "v_cvt_pk_bf16_f32 v22, v148, v149\n\t"
      "v_mfma_f32_16x16x32_bf16 v[252:255], a[116:119], v[28:31], v[252:255]\n\t"
      "v_cvt_pk_bf16_f32 v23, v150, v151\n\t"
      "s_waitcnt lgkmcnt(0)\n\t"
      "s_nop 0\n\t"
      "v_mfma_f32_16x16x32_bf16 a[120:123], a[0:3], v[16:19], a[120:123]\n\t"
      "ds_read_b128 a[60:63], v155 offset:0\n\t"
      "v_lshrrev_b32 v139, 8, v62\n\t"
      "v_lshrrev_b32 v143, 8, v63\n\t"
      "v_mfma_f32_16x16x32_bf16 a[124:127], a[0:3], v[20:23], a[124:127]\n\t"
      "s_add_u32 m0, s19, 135168\n\t"
      "ds_read_b128 a[64:67], v155 offset:2048\n\t"
      "v_and_b32_e64 v136, v11, v62\n\t"
      "v_mfma_f32_16x16x32_bf16 a[128:131], a[4:7], v[16:19], a[128:131]\n\t"
      "buffer_load_dwordx4 v5, s[20:23], s44 offen offset:0 lds\n\t"
      "ds_read_b128 a[68:71], v155 offset:4096\n\t"
      "v_and_b32 v137, v12, v62\n\t"
      "v_and_b32 v140, v11, v63\n\t"
      "v_mfma_f32_16x16x32_bf16 a[132:135], a[4:7], v[20:23], a[132:135]\n\t"
      "buffer_load_dwordx2 v[98:99], v14, s[24:27], s60 offen\n\t"
      "v_and_b32 v141, v12, v63\n\t"
      "v_and_b32 v138, v11, v139\n\t"
      "v_mfma_f32_16x16x32_bf16 a[136:139], a[8:11], v[16:19], a[136:139]\n\t"
      "ds_read_b128 a[72:75], v155 offset:6144\n\t"
      "v_and_b32 v139, v12, v139\n\t"
      "v_and_b32 v142, v11, v143\n\t"
      "v_and_b32_e64 v143, v12, v143\n\t"
      "v_mfma_f32_16x16x32_bf16 a[140:143], a[8:11], v[20:23], a[140:143]\n\t"
      "ds_read_b128 a[76:79], v155 offset:8192\n\t"
      "v_fma_mix_f32 v144, v136, v130, v134 op_sel_hi:[1,0,0]\n\t"
      "v_fma_mix_f32 v145, v137, v132, v134 op_sel_hi:[1,0,0]\n\t"
      "v_mfma_f32_16x16x32_bf16 v[160:163], a[12:15], v[16:19], v[160:163]\n\t"
      "ds_read_b128 a[80:83], v155 offset:10240\n\t"
      "v_fma_mix_f32 v146, v138, v130, v134 op_sel_hi:[1,0,0]\n\t"
      "v_fma_mix_f32 v147, v139, v132, v134 op_sel_hi:[1,0,0]\n\t"
      "v_mfma_f32_16x16x32_bf16 v[164:167], a[12:15], v[20:23], v[164:167]\n\t"
      "buffer_load_dwordx4 v6, s[20:23], s44 offen offset:1024 lds\n\t"
      "v_fma_mix_f32 v148, v136, v130, v134 op_sel:[1,0,0] op_sel_hi:[1,0,0]\n\t"
      "v_fma_mix_f32 v149, v137, v132, v134 op_sel:[1,0,0] op_sel_hi:[1,0,0]\n\t"
      "v_cvt_pk_bf16_f32 v56, v144, v145\n\t"
      "v_mfma_f32_16x16x32_bf16 v[168:171], a[16:19], v[16:19], v[168:171]\n\t"
      "buffer_load_dword v84, v15, s[28:31], s87 offen\n\t"
      "ds_read_b128 a[84:87], v155 offset:12288\n\t"
      "v_cvt_pk_bf16_f32 v57, v146, v147\n\t"
      "v_mfma_f32_16x16x32_bf16 v[172:175], a[16:19], v[20:23], v[172:175]\n\t"
      "ds_read_b128 a[88:91], v155 offset:14336\n\t"
      "v_fma_mix_f32 v150, v138, v130, v134 op_sel:[1,0,0] op_sel_hi:[1,0,0]\n\t"
      "v_fma_mix_f32 v151, v139, v132, v134 op_sel:[1,0,0] op_sel_hi:[1,0,0]\n\t"
      "v_cvt_pk_bf16_f32 v58, v148, v149\n\t"
      "v_mfma_f32_16x16x32_bf16 v[176:179], a[20:23], v[16:19], v[176:179]\n\t"
      "ds_read_b128 a[92:95], v155 offset:16384\n\t"
      "v_cvt_pk_bf16_f32 v59, v150, v151\n\t"
      "v_fma_mix_f32 v144, v140, v131, v135 op_sel_hi:[1,0,0]\n\t"
      "v_mfma_f32_16x16x32_bf16 v[180:183], a[20:23], v[20:23], v[180:183]\n\t"
      "v_fma_mix_f32 v145, v141, v133, v135 op_sel_hi:[1,0,0]\n\t"
      "v_fma_mix_f32 v146, v142, v131, v135 op_sel_hi:[1,0,0]\n\t"
      "v_fma_mix_f32 v147, v143, v133, v135 op_sel_hi:[1,0,0]\n\t"
      "v_mfma_f32_16x16x32_bf16 v[184:187], a[24:27], v[16:19], v[184:187]\n\t"
      "buffer_load_dwordx4 v7, s[20:23], s44 offen offset:2048 lds\n\t"
      "ds_read_b128 a[96:99], v155 offset:18432\n\t"
      "v_fma_mix_f32 v148, v140, v131, v135 op_sel:[1,0,0] op_sel_hi:[1,0,0]\n\t"
      "v_fma_mix_f32 v149, v141, v133, v135 op_sel:[1,0,0] op_sel_hi:[1,0,0]\n\t"
      "v_mfma_f32_16x16x32_bf16 v[188:191], a[24:27], v[20:23], v[188:191]\n\t"
      "buffer_load_dword v85, v15, s[4:7], s87 offen\n\t"
      "ds_read_b128 a[100:103], v155 offset:20480\n\t"
      "v_cvt_pk_bf16_f32 v60, v144, v145\n\t"
      "v_mfma_f32_16x16x32_bf16 v[192:195], a[28:31], v[16:19], v[192:195]\n\t"
      "ds_read_b128 a[104:107], v155 offset:22528\n\t"
      "v_cvt_pk_bf16_f32 v61, v146, v147\n\t"
      "v_fma_mix_f32 v150, v142, v131, v135 op_sel:[1,0,0] op_sel_hi:[1,0,0]\n\t"
      "v_fma_mix_f32 v151, v143, v133, v135 op_sel:[1,0,0] op_sel_hi:[1,0,0]\n\t"
      "v_mfma_f32_16x16x32_bf16 v[196:199], a[28:31], v[20:23], v[196:199]\n\t"
      "v_cvt_pk_bf16_f32 v62, v148, v149\n\t"
      "v_cvt_pk_bf16_f32 v63, v150, v151\n\t"
      "v_lshrrev_b32_e64 v139, 8, v106\n\t"
      "v_mfma_f32_16x16x32_bf16 v[200:203], a[32:35], v[16:19], v[200:203]\n\t"
      "ds_read_b128 a[108:111], v155 offset:24576\n\t"
      "v_lshrrev_b32 v143, 8, v107\n\t"
      "v_and_b32 v136, v11, v106\n\t"
      "v_and_b32_e64 v137, v12, v106\n\t"
      "v_mfma_f32_16x16x32_bf16 v[204:207], a[32:35], v[20:23], v[204:207]\n\t"
      "ds_read_b128 a[112:115], v155 offset:26624\n\t"
      "v_and_b32 v140, v11, v107\n\t"
      "v_and_b32 v141, v12, v107\n\t"
      "v_mfma_f32_16x16x32_bf16 v[208:211], a[36:39], v[16:19], v[208:211]\n\t"
      "buffer_load_dwordx4 v8, s[20:23], s44 offen offset:3072 lds\n\t"
      "ds_read_b128 a[116:119], v155 offset:28672\n\t"
      "v_and_b32_e64 v138, v11, v139\n\t"
      "v_mfma_f32_16x16x32_bf16 v[212:215], a[36:39], v[20:23], v[212:215]\n\t"
      "buffer_load_dwordx2 v[86:87], v13, s[24:27], s71 offen\n\t"
      "v_and_b32 v139, v12, v139\n\t"
      "v_and_b32 v142, v11, v143\n\t"
      "v_and_b32_e64 v143, v12, v143\n\t"
      "v_mfma_f32_16x16x32_bf16 v[216:219], a[40:43], v[16:19], v[216:219]\n\t"
      "v_lshlrev_b32 v120, 16, v118\n\t"
      "v_and_b32 v121, v10, v118\n\t"
      "v_lshlrev_b32_e64 v122, 16, v119\n\t"
      "v_mfma_f32_16x16x32_bf16 v[220:223], a[40:43], v[20:23], v[220:223]\n\t"
      "v_and_b32_e64 v123, v10, v119\n\t"
      "v_fma_mix_f32 v144, v136, v124, v128 op_sel_hi:[1,0,0]\n\t"
      "v_fma_mix_f32 v145, v137, v126, v128 op_sel_hi:[1,0,0]\n\t"
      "v_fma_mix_f32 v146, v138, v124, v128 op_sel_hi:[1,0,0]\n\t"
      "v_mfma_f32_16x16x32_bf16 v[224:227], a[44:47], v[16:19], v[224:227]\n\t"
      "s_add_u32 m0, s19, 0\n\t"
      "s_nop 0\n\t"
      "v_fma_mix_f32 v147, v139, v126, v128 op_sel_hi:[1,0,0]\n\t"
      "v_fma_mix_f32 v148, v136, v124, v128 op_sel:[1,0,0] op_sel_hi:[1,0,0]\n\t"
      "v_mfma_f32_16x16x32_bf16 v[228:231], a[44:47], v[20:23], v[228:231]\n\t"
      "buffer_load_dwordx4 v1, s[20:23], s45 offen offset:0 lds\n\t"
      "v_fma_mix_f32 v149, v137, v126, v128 op_sel:[1,0,0] op_sel_hi:[1,0,0]\n\t"
      "v_cvt_pk_bf16_f32 v24, v144, v145\n\t"
      "v_mfma_f32_16x16x32_bf16 v[232:235], a[48:51], v[16:19], v[232:235]\n\t"
      "buffer_load_dwordx2 v[94:95], v14, s[24:27], s71 offen\n\t"
      "v_cvt_pk_bf16_f32 v25, v146, v147\n\t"
      "v_fma_mix_f32 v150, v138, v124, v128 op_sel:[1,0,0] op_sel_hi:[1,0,0]\n\t"
      "v_fma_mix_f32 v151, v139, v126, v128 op_sel:[1,0,0] op_sel_hi:[1,0,0]\n\t"
      "v_mfma_f32_16x16x32_bf16 v[236:239], a[48:51], v[20:23], v[236:239]\n\t"
      "v_cvt_pk_bf16_f32 v26, v148, v149\n\t"
      "v_cvt_pk_bf16_f32 v27, v150, v151\n\t"
      "v_fma_mix_f32 v144, v140, v125, v129 op_sel_hi:[1,0,0]\n\t"
      "v_mfma_f32_16x16x32_bf16 v[240:243], a[52:55], v[16:19], v[240:243]\n\t"
      "v_fma_mix_f32 v145, v141, v127, v129 op_sel_hi:[1,0,0]\n\t"
      "v_fma_mix_f32 v146, v142, v125, v129 op_sel_hi:[1,0,0]\n\t"
      "v_fma_mix_f32 v147, v143, v127, v129 op_sel_hi:[1,0,0]\n\t"
      "v_fma_mix_f32 v148, v140, v125, v129 op_sel:[1,0,0] op_sel_hi:[1,0,0]\n\t"
      "v_mfma_f32_16x16x32_bf16 v[244:247], a[52:55], v[20:23], v[244:247]\n\t"
      "v_fma_mix_f32 v149, v141, v127, v129 op_sel:[1,0,0] op_sel_hi:[1,0,0]\n\t"
      "v_cvt_pk_bf16_f32 v28, v144, v145\n\t"
      "v_cvt_pk_bf16_f32 v29, v146, v147\n\t"
      "v_mfma_f32_16x16x32_bf16 v[248:251], a[56:59], v[16:19], v[248:251]\n\t"
      "v_fma_mix_f32 v150, v142, v125, v129 op_sel:[1,0,0] op_sel_hi:[1,0,0]\n\t"
      "v_fma_mix_f32 v151, v143, v127, v129 op_sel:[1,0,0] op_sel_hi:[1,0,0]\n\t"
      "v_cvt_pk_bf16_f32 v30, v148, v149\n\t"
      "v_cvt_pk_bf16_f32 v31, v150, v151\n\t"
      "v_mfma_f32_16x16x32_bf16 v[252:255], a[56:59], v[20:23], v[252:255]\n\t"
      "s_waitcnt vmcnt(17) lgkmcnt(0)\n\t"
      "s_barrier\n\t"
      "v_mfma_f32_16x16x32_bf16 a[120:123], a[60:63], v[24:27], a[120:123]\n\t"
      "ds_read_b128 a[0:3], v154 offset:32768\n\t"
      "v_lshlrev_b32 v144, 16, v68\n\t"
      "v_and_b32 v145, v10, v68\n\t"
      "v_mfma_f32_16x16x32_bf16 a[124:127], a[60:63], v[28:31], a[124:127]\n\t"
      "ds_read_b128 a[4:7], v154 offset:34816\n\t"
      "v_lshlrev_b32 v146, 16, v69\n\t"
      "v_and_b32 v147, v10, v69\n\t"
      "v_mul_f32 v130, 0x4b800000, v144\n\t"
      "v_mfma_f32_16x16x32_bf16 a[128:131], a[64:67], v[24:27], a[128:131]\n\t"
      "buffer_load_dwordx4 v2, s[20:23], s45 offen offset:1024 lds\n\t"
      "ds_read_b128 a[8:11], v154 offset:36864\n\t"
      "v_mul_f32 v132, 0x49800000, v144\n\t"
      "v_mul_f32_e64 v134, -v146, v144\n\t"
      "v_mfma_f32_16x16x32_bf16 a[132:135], a[64:67], v[28:31], a[132:135]\n\t"
      "buffer_load_dwordx2 v[100:101], v13, s[24:27], s61 offen\n\t"
      "v_mul_f32 v131, 0x4b800000, v145\n\t"
      "v_mul_f32 v133, 0x49800000, v145\n\t"
      "v_mfma_f32_16x16x32_bf16 a[136:139], a[68:71], v[24:27], a[136:139]\n\t"
      "ds_read_b128 a[12:15], v154 offset:38912\n\t"
      "v_mul_f32_e64 v135, -v147, v145\n\t"
      "v_lshrrev_b32 v139, 8, v70\n\t"
      "v_lshrrev_b32 v143, 8, v71\n\t"
      "v_mfma_f32_16x16x32_bf16 a[140:143], a[68:71], v[28:31], a[140:143]\n\t"
      "ds_read_b128 a[16:19], v154 offset:40960\n\t"
      "v_and_b32 v136, v11, v70\n\t"
      "v_and_b32 v137, v12, v70\n\t"
      "v_and_b32_e64 v140, v11, v71\n\t"
      "v_mfma_f32_16x16x32_bf16 v[160:163], a[72:75], v[24:27], v[160:163]\n\t"
      "ds_read_b128 a[20:23], v154 offset:43008\n\t"
      "v_and_b32 v141, v12, v71\n\t"
      "v_and_b32 v138, v11, v139\n\t"
      "v_mfma_f32_16x16x32_bf16 v[164:167], a[72:75], v[28:31], v[164:167]\n\t"
      "buffer_load_dwordx4 v3, s[20:23], s45 offen offset:2048 lds\n\t"
      "v_and_b32 v139, v12, v139\n\t"
      "v_and_b32 v142, v11, v143\n\t"
      "v_and_b32_e64 v143, v12, v143\n\t"
      "v_mfma_f32_16x16x32_bf16 v[168:171], a[76:79], v[24:27], v[168:171]\n\t"
      "ds_read_b128 a[24:27], v154 offset:45056\n\t"
      "v_fma_mix_f32 v144, v136, v130, v134 op_sel_hi:[1,0,0]\n\t"
      "v_fma_mix_f32 v145, v137, v132, v134 op_sel_hi:[1,0,0]\n\t"
      "v_fma_mix_f32 v146, v138, v130, v134 op_sel_hi:[1,0,0]\n\t"
      "v_mfma_f32_16x16x32_bf16 v[172:175], a[76:79], v[28:31], v[172:175]\n\t"
      "ds_read_b128 a[28:31], v154 offset:47104\n\t"
      "v_fma_mix_f32 v147, v139, v132, v134 op_sel_hi:[1,0,0]\n\t"
      "v_fma_mix_f32 v148, v136, v130, v134 op_sel:[1,0,0] op_sel_hi:[1,0,0]\n\t"
      "v_mfma_f32_16x16x32_bf16 v[176:179], a[80:83], v[24:27], v[176:179]\n\t"
      "ds_read_b128 a[32:35], v154 offset:49152\n\t"
      "v_fma_mix_f32 v149, v137, v132, v134 op_sel:[1,0,0] op_sel_hi:[1,0,0]\n\t"
      "v_cvt_pk_bf16_f32 v64, v144, v145\n\t"
      "v_cvt_pk_bf16_f32 v65, v146, v147\n\t"
      "v_mfma_f32_16x16x32_bf16 v[180:183], a[80:83], v[28:31], v[180:183]\n\t"
      "buffer_load_dword v116, v15, s[28:31], s78 offen\n\t"
      "v_fma_mix_f32 v150, v138, v130, v134 op_sel:[1,0,0] op_sel_hi:[1,0,0]\n\t"
      "v_fma_mix_f32 v151, v139, v132, v134 op_sel:[1,0,0] op_sel_hi:[1,0,0]\n\t"
      "v_cvt_pk_bf16_f32 v66, v148, v149\n\t"
      "v_mfma_f32_16x16x32_bf16 v[184:187], a[84:87], v[24:27], v[184:187]\n\t"
      "buffer_load_dwordx4 v4, s[20:23], s45 offen offset:3072 lds\n\t"
      "ds_read_b128 a[36:39], v154 offset:51200\n\t"
      "v_cvt_pk_bf16_f32 v67, v150, v151\n\t"
      "v_mfma_f32_16x16x32_bf16 v[188:191], a[84:87], v[28:31], v[188:191]\n\t"
      "ds_read_b128 a[40:43], v154 offset:53248\n\t"
      "v_fma_mix_f32 v144, v140, v131, v135 op_sel_hi:[1,0,0]\n\t"
      "v_fma_mix_f32 v145, v141, v133, v135 op_sel_hi:[1,0,0]\n\t"
      "v_fma_mix_f32 v146, v142, v131, v135 op_sel_hi:[1,0,0]\n\t"
      "v_mfma_f32_16x16x32_bf16 v[192:195], a[88:91], v[24:27], v[192:195]\n\t"
      "ds_read_b128 a[44:47], v154 offset:55296\n\t"
      "v_fma_mix_f32 v147, v143, v133, v135 op_sel_hi:[1,0,0]\n\t"
      "v_fma_mix_f32 v148, v140, v131, v135 op_sel:[1,0,0] op_sel_hi:[1,0,0]\n\t"
      "v_fma_mix_f32 v149, v141, v133, v135 op_sel:[1,0,0] op_sel_hi:[1,0,0]\n\t"
      "v_mfma_f32_16x16x32_bf16 v[196:199], a[88:91], v[28:31], v[196:199]\n\t"
      "v_cvt_pk_bf16_f32 v68, v144, v145\n\t"
      "v_cvt_pk_bf16_f32 v69, v146, v147\n\t"
      "v_fma_mix_f32 v150, v142, v131, v135 op_sel:[1,0,0] op_sel_hi:[1,0,0]\n\t"
      "v_fma_mix_f32 v151, v143, v133, v135 op_sel:[1,0,0] op_sel_hi:[1,0,0]\n\t"
      "v_mfma_f32_16x16x32_bf16 v[200:203], a[92:95], v[24:27], v[200:203]\n\t"
      "ds_read_b128 a[48:51], v154 offset:57344\n\t"
      "v_cvt_pk_bf16_f32 v70, v148, v149\n\t"
      "v_cvt_pk_bf16_f32 v71, v150, v151\n\t"
      "v_mfma_f32_16x16x32_bf16 v[204:207], a[92:95], v[28:31], v[204:207]\n\t"
      "s_add_u32 m0, s19, 4096\n\t"
      "ds_read_b128 a[52:55], v154 offset:59392\n\t"
      "v_lshrrev_b32 v139, 8, v108\n\t"
      "v_lshrrev_b32 v143, 8, v109\n\t"
      "v_mfma_f32_16x16x32_bf16 v[208:211], a[96:99], v[24:27], v[208:211]\n\t"
      "buffer_load_dwordx4 v5, s[20:23], s45 offen offset:0 lds\n\t"
      "ds_read_b128 a[56:59], v154 offset:61440\n\t"
      "v_and_b32 v136, v11, v108\n\t"
      "v_and_b32 v137, v12, v108\n\t"
      "v_mfma_f32_16x16x32_bf16 v[212:215], a[96:99], v[28:31], v[212:215]\n\t"
      "buffer_load_dword v117, v15, s[4:7], s78 offen\n\t"
      "v_and_b32 v140, v11, v109\n\t"
      "v_and_b32 v141, v12, v109\n\t"
      "v_mfma_f32_16x16x32_bf16 v[216:219], a[100:103], v[24:27], v[216:219]\n\t"
      "v_and_b32 v138, v11, v139\n\t"
      "v_and_b32 v139, v12, v139\n\t"
      "v_and_b32 v142, v11, v143\n\t"
      "v_and_b32 v143, v12, v143\n\t"
      "v_mfma_f32_16x16x32_bf16 v[220:223], a[100:103], v[28:31], v[220:223]\n\t"
      "v_mul_f32 v124, 0x4b800000, v120\n\t"
      "v_mul_f32 v126, 0x49800000, v120\n\t"
      "v_mul_f32_e64 v128, -v122, v120\n\t"
      "v_mul_f32 v125, 0x4b800000, v121\n\t"
      "v_mfma_f32_16x16x32_bf16 v[224:227], a[104:107], v[24:27], v[224:227]\n\t"
      "v_mul_f32 v127, 0x49800000, v121\n\t"
      "v_mul_f32_e64 v129, -v123, v121\n\t"
      "v_fma_mix_f32 v144, v136, v124, v128 op_sel_hi:[1,0,0]\n\t"
      "v_mfma_f32_16x16x32_bf16 v[228:231], a[104:107], v[28:31], v[228:231]\n\t"
      "buffer_load_dwordx4 v6, s[20:23], s45 offen offset:1024 lds\n\t"
      "v_fma_mix_f32 v145, v137, v126, v128 op_sel_hi:[1,0,0]\n\t"
      "v_fma_mix_f32 v146, v138, v124, v128 op_sel_hi:[1,0,0]\n\t"
      "v_fma_mix_f32 v147, v139, v126, v128 op_sel_hi:[1,0,0]\n\t"
      "v_mfma_f32_16x16x32_bf16 v[232:235], a[108:111], v[24:27], v[232:235]\n\t"
      "v_fma_mix_f32 v148, v136, v124, v128 op_sel:[1,0,0] op_sel_hi:[1,0,0]\n\t"
      "v_fma_mix_f32 v149, v137, v126, v128 op_sel:[1,0,0] op_sel_hi:[1,0,0]\n\t"
      "v_cvt_pk_bf16_f32 v16, v144, v145\n\t"
      "v_cvt_pk_bf16_f32 v17, v146, v147\n\t"
      "v_mfma_f32_16x16x32_bf16 v[236:239], a[108:111], v[28:31], v[236:239]\n\t"
      "v_fma_mix_f32 v150, v138, v124, v128 op_sel:[1,0,0] op_sel_hi:[1,0,0]\n\t"
      "v_fma_mix_f32 v151, v139, v126, v128 op_sel:[1,0,0] op_sel_hi:[1,0,0]\n\t"
      "v_cvt_pk_bf16_f32 v18, v148, v149\n\t"
      "v_mfma_f32_16x16x32_bf16 v[240:243], a[112:115], v[24:27], v[240:243]\n\t"
      "v_cvt_pk_bf16_f32 v19, v150, v151\n\t"
      "v_fma_mix_f32 v144, v140, v125, v129 op_sel_hi:[1,0,0]\n\t"
      "v_fma_mix_f32 v145, v141, v127, v129 op_sel_hi:[1,0,0]\n\t"
      "v_fma_mix_f32 v146, v142, v125, v129 op_sel_hi:[1,0,0]\n\t"
      "v_mfma_f32_16x16x32_bf16 v[244:247], a[112:115], v[28:31], v[244:247]\n\t"
      "v_fma_mix_f32 v147, v143, v127, v129 op_sel_hi:[1,0,0]\n\t"
      "v_fma_mix_f32 v148, v140, v125, v129 op_sel:[1,0,0] op_sel_hi:[1,0,0]\n\t"
      "v_fma_mix_f32 v149, v141, v127, v129 op_sel:[1,0,0] op_sel_hi:[1,0,0]\n\t"
      "v_cvt_pk_bf16_f32 v20, v144, v145\n\t"
      "v_mfma_f32_16x16x32_bf16 v[248:251], a[116:119], v[24:27], v[248:251]\n\t"
      "v_cvt_pk_bf16_f32 v21, v146, v147\n\t"
      "v_fma_mix_f32 v150, v142, v125, v129 op_sel:[1,0,0] op_sel_hi:[1,0,0]\n\t"
      "v_fma_mix_f32 v151, v143, v127, v129 op_sel:[1,0,0] op_sel_hi:[1,0,0]\n\t"
      "v_cvt_pk_bf16_f32 v22, v148, v149\n\t"
      "v_mfma_f32_16x16x32_bf16 v[252:255], a[116:119], v[28:31], v[252:255]\n\t"
      "v_cvt_pk_bf16_f32 v23, v150, v151\n\t"
      "s_waitcnt lgkmcnt(0)\n\t"
      "s_nop 0\n\t"
      "v_mfma_f32_16x16x32_bf16 a[120:123], a[0:3], v[16:19], a[120:123]\n\t"
      "ds_read_b128 a[60:63], v155 offset:32768\n\t"
      "v_lshrrev_b32 v139, 8, v78\n\t"
      "v_lshrrev_b32 v143, 8, v79\n\t"
      "v_mfma_f32_16x16x32_bf16 a[124:127], a[0:3], v[20:23], a[124:127]\n\t"
      "ds_read_b128 a[64:67], v155 offset:34816\n\t"
      "v_and_b32 v136, v11, v78\n\t"
      "v_and_b32 v137, v12, v78\n\t"
      "v_mfma_f32_16x16x32_bf16 a[128:131], a[4:7], v[16:19], a[128:131]\n\t"
      "buffer_load_dwordx4 v7, s[20:23], s45 offen offset:2048 lds\n\t"
      "ds_read_b128 a[68:71], v155 offset:36864\n\t"
      "v_and_b32_e64 v140, v11, v79\n\t"
      "v_mfma_f32_16x16x32_bf16 a[132:135], a[4:7], v[20:23], a[132:135]\n\t"
      "buffer_load_dwordx2 v[102:103], v14, s[24:27], s61 offen\n\t"
      "v_and_b32 v141, v12, v79\n\t"
      "v_and_b32 v138, v11, v139\n\t"
      "v_mfma_f32_16x16x32_bf16 a[136:139], a[8:11], v[16:19], a[136:139]\n\t"
      "ds_read_b128 a[72:75], v155 offset:38912\n\t"
      "v_and_b32 v139, v12, v139\n\t"
      "v_and_b32 v142, v11, v143\n\t"
      "v_and_b32_e64 v143, v12, v143\n\t"
      "v_mfma_f32_16x16x32_bf16 a[140:143], a[8:11], v[20:23], a[140:143]\n\t"
      "ds_read_b128 a[76:79], v155 offset:40960\n\t"
      "v_fma_mix_f32 v144, v136, v130, v134 op_sel_hi:[1,0,0]\n\t"
      "v_fma_mix_f32 v145, v137, v132, v134 op_sel_hi:[1,0,0]\n\t"
      "v_mfma_f32_16x16x32_bf16 v[160:163], a[12:15], v[16:19], v[160:163]\n\t"
      "ds_read_b128 a[80:83], v155 offset:43008\n\t"
      "v_fma_mix_f32 v146, v138, v130, v134 op_sel_hi:[1,0,0]\n\t"
      "v_fma_mix_f32 v147, v139, v132, v134 op_sel_hi:[1,0,0]\n\t"
      "v_mfma_f32_16x16x32_bf16 v[164:167], a[12:15], v[20:23], v[164:167]\n\t"
      "buffer_load_dwordx4 v8, s[20:23], s45 offen offset:3072 lds\n\t"
      "v_fma_mix_f32 v148, v136, v130, v134 op_sel:[1,0,0] op_sel_hi:[1,0,0]\n\t"
      "v_fma_mix_f32 v149, v137, v132, v134 op_sel:[1,0,0] op_sel_hi:[1,0,0]\n\t"
      "v_mfma_f32_16x16x32_bf16 v[168:171], a[16:19], v[16:19], v[168:171]\n\t"
      "ds_read_b128 a[84:87], v155 offset:45056\n\t"
      "v_cvt_pk_bf16_f32 v72, v144, v145\n\t"
      "v_cvt_pk_bf16_f32 v73, v146, v147\n\t"
      "v_fma_mix_f32 v150, v138, v130, v134 op_sel:[1,0,0] op_sel_hi:[1,0,0]\n\t"
      "v_mfma_f32_16x16x32_bf16 v[172:175], a[16:19], v[20:23], v[172:175]\n\t"
      "ds_read_b128 a[88:91], v155 offset:47104\n\t"
      "v_fma_mix_f32 v151, v139, v132, v134 op_sel:[1,0,0] op_sel_hi:[1,0,0]\n\t"
      "v_cvt_pk_bf16_f32 v74, v148, v149\n\t"
      "v_mfma_f32_16x16x32_bf16 v[176:179], a[20:23], v[16:19], v[176:179]\n\t"
      "ds_read_b128 a[92:95], v155 offset:49152\n\t"
      "v_cvt_pk_bf16_f32 v75, v150, v151\n\t"
      "v_fma_mix_f32 v144, v140, v131, v135 op_sel_hi:[1,0,0]\n\t"
      "v_mfma_f32_16x16x32_bf16 v[180:183], a[20:23], v[20:23], v[180:183]\n\t"
      "s_add_u32 m0, s19, 32768\n\t"
      "v_fma_mix_f32 v145, v141, v133, v135 op_sel_hi:[1,0,0]\n\t"
      "v_fma_mix_f32 v146, v142, v131, v135 op_sel_hi:[1,0,0]\n\t"
      "v_mfma_f32_16x16x32_bf16 v[184:187], a[24:27], v[16:19], v[184:187]\n\t"
      "buffer_load_dwordx4 v1, s[20:23], s46 offen offset:0 lds\n\t"
      "ds_read_b128 a[96:99], v155 offset:51200\n\t"
      "v_fma_mix_f32 v147, v143, v133, v135 op_sel_hi:[1,0,0]\n\t"
      "v_fma_mix_f32 v148, v140, v131, v135 op_sel:[1,0,0] op_sel_hi:[1,0,0]\n\t"
      "v_mfma_f32_16x16x32_bf16 v[188:191], a[24:27], v[20:23], v[188:191]\n\t"
      "ds_read_b128 a[100:103], v155 offset:53248\n\t"
      "v_fma_mix_f32 v149, v141, v133, v135 op_sel:[1,0,0] op_sel_hi:[1,0,0]\n\t"
      "v_cvt_pk_bf16_f32 v76, v144, v145\n\t"
      "v_mfma_f32_16x16x32_bf16 v[192:195], a[28:31], v[16:19], v[192:195]\n\t"
      "ds_read_b128 a[104:107], v155 offset:55296\n\t"
      "v_cvt_pk_bf16_f32 v77, v146, v147\n\t"
      "v_fma_mix_f32 v150, v142, v131, v135 op_sel:[1,0,0] op_sel_hi:[1,0,0]\n\t"
      "v_mfma_f32_16x16x32_bf16 v[196:199], a[28:31], v[20:23], v[196:199]\n\t"
      "v_fma_mix_f32 v151, v143, v133, v135 op_sel:[1,0,0] op_sel_hi:[1,0,0]\n\t"
      "v_cvt_pk_bf16_f32 v78, v148, v149\n\t"
      "v_cvt_pk_bf16_f32 v79, v150, v151\n\t"
      "v_mfma_f32_16x16x32_bf16 v[200:203], a[32:35], v[16:19], v[200:203]\n\t"
      "ds_read_b128 a[108:111], v155 offset:57344\n\t"
      "v_lshrrev_b32 v139, 8, v110\n\t"
      "v_lshrrev_b32 v143, 8, v111\n\t"
      "v_and_b32_e64 v136, v11, v110\n\t"
      "v_mfma_f32_16x16x32_bf16 v[204:207], a[32:35], v[20:23], v[204:207]\n\t"
      "ds_read_b128 a[112:115], v155 offset:59392\n\t"
      "v_and_b32 v137, v12, v110\n\t"
      "v_and_b32 v140, v11, v111\n\t"
      "v_mfma_f32_16x16x32_bf16 v[208:211], a[36:39], v[16:19], v[208:211]\n\t"
      "buffer_load_dwordx4 v2, s[20:23], s46 offen offset:1024 lds\n\t"
      "ds_read_b128 a[116:119], v155 offset:61440\n\t"
      "v_and_b32_e64 v141, v12, v111\n\t"
      "v_mfma_f32_16x16x32_bf16 v[212:215], a[36:39], v[20:23], v[212:215]\n\t"
      "v_and_b32 v138, v11, v139\n\t"
      "v_and_b32 v139, v12, v139\n\t"
      "v_and_b32_e64 v142, v11, v143\n\t"
      "v_mfma_f32_16x16x32_bf16 v[216:219], a[40:43], v[16:19], v[216:219]\n\t"
      "v_and_b32 v143, v12, v143\n\t"
      "v_lshlrev_b32 v120, 16, v112\n\t"
      "v_and_b32 v121, v10, v112\n\t"
      "v_lshlrev_b32 v122, 16, v113\n\t"
      "v_mfma_f32_16x16x32_bf16 v[220:223], a[40:43], v[20:23], v[220:223]\n\t"
      "v_and_b32_e64 v123, v10, v113\n\t"
      "v_fma_mix_f32 v144, v136, v124, v128 op_sel_hi:[1,0,0]\n\t"
      "v_fma_mix_f32 v145, v137, v126, v128 op_sel_hi:[1,0,0]\n\t"
      "v_mfma_f32_16x16x32_bf16 v[224:227], a[44:47], v[16:19], v[224:227]\n\t"
      "v_fma_mix_f32 v146, v138, v124, v128 op_sel_hi:[1,0,0]\n\t"
      "v_fma_mix_f32 v147, v139, v126, v128 op_sel_hi:[1,0,0]\n\t"
      "v_fma_mix_f32 v148, v136, v124, v128 op_sel:[1,0,0] op_sel_hi:[1,0,0]\n\t"
      "v_mfma_f32_16x16x32_bf16 v[228:231], a[44:47], v[20:23], v[228:231]\n\t"
      "buffer_load_dwordx4 v3, s[20:23], s46 offen offset:2048 lds\n\t"
      "v_fma_mix_f32 v149, v137, v126, v128 op_sel:[1,0,0] op_sel_hi:[1,0,0]\n\t"
      "v_cvt_pk_bf16_f32 v24, v144, v145\n\t"
      "v_mfma_f32_16x16x32_bf16 v[232:235], a[48:51], v[16:19], v[232:235]\n\t"
      "v_cvt_pk_bf16_f32 v25, v146, v147\n\t"
      "v_fma_mix_f32 v150, v138, v124, v128 op_sel:[1,0,0] op_sel_hi:[1,0,0]\n\t"
      "v_fma_mix_f32 v151, v139, v126, v128 op_sel:[1,0,0] op_sel_hi:[1,0,0]\n\t"
      "v_cvt_pk_bf16_f32 v26, v148, v149\n\t"
      "v_mfma_f32_16x16x32_bf16 v[236:239], a[48:51], v[20:23], v[236:239]\n\t"
      "v_cvt_pk_bf16_f32 v27, v150, v151\n\t"
      "v_fma_mix_f32 v144, v140, v125, v129 op_sel_hi:[1,0,0]\n\t"
      "v_fma_mix_f32 v145, v141, v127, v129 op_sel_hi:[1,0,0]\n\t"
      "v_mfma_f32_16x16x32_bf16 v[240:243], a[52:55], v[16:19], v[240:243]\n\t"
      "v_fma_mix_f32 v146, v142, v125, v129 op_sel_hi:[1,0,0]\n\t"
      "v_fma_mix_f32 v147, v143, v127, v129 op_sel_hi:[1,0,0]\n\t"
      "v_fma_mix_f32 v148, v140, v125, v129 op_sel:[1,0,0] op_sel_hi:[1,0,0]\n\t"
      "v_mfma_f32_16x16x32_bf16 v[244:247], a[52:55], v[20:23], v[244:247]\n\t"
      "v_fma_mix_f32 v149, v141, v127, v129 op_sel:[1,0,0] op_sel_hi:[1,0,0]\n\t"
      "v_cvt_pk_bf16_f32 v28, v144, v145\n\t"
      "v_cvt_pk_bf16_f32 v29, v146, v147\n\t"
      "v_mfma_f32_16x16x32_bf16 v[248:251], a[56:59], v[16:19], v[248:251]\n\t"
      "v_fma_mix_f32 v150, v142, v125, v129 op_sel:[1,0,0] op_sel_hi:[1,0,0]\n\t"
      "v_fma_mix_f32 v151, v143, v127, v129 op_sel:[1,0,0] op_sel_hi:[1,0,0]\n\t"
      "v_cvt_pk_bf16_f32 v30, v148, v149\n\t"
      "v_cvt_pk_bf16_f32 v31, v150, v151\n\t"
      "v_mfma_f32_16x16x32_bf16 v[252:255], a[56:59], v[20:23], v[252:255]\n\t"
      "s_waitcnt vmcnt(14) lgkmcnt(0)\n\t"
      "s_barrier\n\t"
      "v_mfma_f32_16x16x32_bf16 a[120:123], a[60:63], v[24:27], a[120:123]\n\t"
      "ds_read_b128 a[0:3], v156 offset:0\n\t"
      "v_lshlrev_b32 v144, 16, v84\n\t"
      "v_and_b32 v145, v10, v84\n\t"
      "v_mfma_f32_16x16x32_bf16 a[124:127], a[60:63], v[28:31], a[124:127]\n\t"
      "ds_read_b128 a[4:7], v156 offset:2048\n\t"
      "v_lshlrev_b32 v146, 16, v85\n\t"
      "v_and_b32 v147, v10, v85\n\t"
      "v_mul_f32 v130, 0x4b800000, v144\n\t"
      "v_mfma_f32_16x16x32_bf16 a[128:131], a[64:67], v[24:27], a[128:131]\n\t"
      "buffer_load_dwordx4 v4, s[20:23], s46 offen offset:3072 lds\n\t"
      "ds_read_b128 a[8:11], v156 offset:4096\n\t"
      "v_mul_f32 v132, 0x49800000, v144\n\t"
      "v_mul_f32_e64 v134, -v146, v144\n\t"
      "v_mfma_f32_16x16x32_bf16 a[132:135], a[64:67], v[28:31], a[132:135]\n\t"
      "buffer_load_dwordx2 v[104:105], v13, s[24:27], s62 offen\n\t"
      "v_mul_f32 v131, 0x4b800000, v145\n\t"
      "v_mul_f32 v133, 0x49800000, v145\n\t"
      "v_mfma_f32_16x16x32_bf16 a[136:139], a[68:71], v[24:27], a[136:139]\n\t"
      "ds_read_b128 a[12:15], v156 offset:6144\n\t"
      "v_mul_f32_e64 v135, -v147, v145\n\t"
      "v_lshrrev_b32 v139, 8, v86\n\t"
      "v_lshrrev_b32 v143, 8, v87\n\t"
      "v_mfma_f32_16x16x32_bf16 a[140:143], a[68:71], v[28:31], a[140:143]\n\t"
      "ds_read_b128 a[16:19], v156 offset:8192\n\t"
      "v_and_b32 v136, v11, v86\n\t"
      "v_and_b32 v137, v12, v86\n\t"
      "v_and_b32_e64 v140, v11, v87\n\t"
      "v_mfma_f32_16x16x32_bf16 v[160:163], a[72:75], v[24:27], v[160:163]\n\t"
      "s_add_u32 m0, s19, 36864\n\t"
      "ds_read_b128 a[20:23], v156 offset:10240\n\t"
      "v_and_b32_e64 v141, v12, v87\n\t"
      "v_mfma_f32_16x16x32_bf16 v[164:167], a[72:75], v[28:31], v[164:167]\n\t"
      "buffer_load_dwordx4 v5, s[20:23], s46 offen offset:0 lds\n\t"
      "v_and_b32 v138, v11, v139\n\t"
      "v_and_b32 v139, v12, v139\n\t"
      "v_and_b32_e64 v142, v11, v143\n\t"
      "v_mfma_f32_16x16x32_bf16 v[168:171], a[76:79], v[24:27], v[168:171]\n\t"
      "ds_read_b128 a[24:27], v156 offset:12288\n\t"
      "v_and_b32_e64 v143, v12, v143\n\t"
      "v_fma_mix_f32 v144, v136, v130, v134 op_sel_hi:[1,0,0]\n\t"
      "v_fma_mix_f32 v145, v137, v132, v134 op_sel_hi:[1,0,0]\n\t"
      "v_mfma_f32_16x16x32_bf16 v[172:175], a[76:79], v[28:31], v[172:175]\n\t"
      "ds_read_b128 a[28:31], v156 offset:14336\n\t"
      "v_fma_mix_f32 v146, v138, v130, v134 op_sel_hi:[1,0,0]\n\t"
      "v_fma_mix_f32 v147, v139, v132, v134 op_sel_hi:[1,0,0]\n\t"
      "v_mfma_f32_16x16x32_bf16 v[176:179], a[80:83], v[24:27], v[176:179]\n\t"
      "ds_read_b128 a[32:35], v156 offset:16384\n\t"
      "v_fma_mix_f32 v148, v136, v130, v134 op_sel:[1,0,0] op_sel_hi:[1,0,0]\n\t"
      "v_fma_mix_f32 v149, v137, v132, v134 op_sel:[1,0,0] op_sel_hi:[1,0,0]\n\t"
      "v_cvt_pk_bf16_f32 v80, v144, v145\n\t"
      "v_mfma_f32_16x16x32_bf16 v[180:183], a[80:83], v[28:31], v[180:183]\n\t"
      "buffer_load_dword v118, v15, s[28:31], s79 offen\n\t"
      "v_cvt_pk_bf16_f32 v81, v146, v147\n\t"
      "v_fma_mix_f32 v150, v138, v130, v134 op_sel:[1,0,0] op_sel_hi:[1,0,0]\n\t"
      "v_fma_mix_f32 v151, v139, v132, v134 op_sel:[1,0,0] op_sel_hi:[1,0,0]\n\t"
      "v_mfma_f32_16x16x32_bf16 v[184:187], a[84:87], v[24:27], v[184:187]\n\t"
      "buffer_load_dwordx4 v6, s[20:23], s46 offen offset:1024 lds\n\t"
      "ds_read_b128 a[36:39], v156 offset:18432\n\t"
      "v_cvt_pk_bf16_f32 v82, v148, v149\n\t"
      "v_mfma_f32_16x16x32_bf16 v[188:191], a[84:87], v[28:31], v[188:191]\n\t"
      "ds_read_b128 a[40:43], v156 offset:20480\n\t"
      "v_cvt_pk_bf16_f32 v83, v150, v151\n\t"
      "v_fma_mix_f32 v144, v140, v131, v135 op_sel_hi:[1,0,0]\n\t"
      "v_fma_mix_f32 v145, v141, v133, v135 op_sel_hi:[1,0,0]\n\t"
      "v_mfma_f32_16x16x32_bf16 v[192:195], a[88:91], v[24:27], v[192:195]\n\t"
      "ds_read_b128 a[44:47], v156 offset:22528\n\t"
      "v_fma_mix_f32 v146, v142, v131, v135 op_sel_hi:[1,0,0]\n\t"
      "v_fma_mix_f32 v147, v143, v133, v135 op_sel_hi:[1,0,0]\n\t"
      "v_fma_mix_f32 v148, v140, v131, v135 op_sel:[1,0,0] op_sel_hi:[1,0,0]\n\t"
      "v_mfma_f32_16x16x32_bf16 v[196:199], a[88:91], v[28:31], v[196:199]\n\t"
      "v_fma_mix_f32 v149, v141, v133, v135 op_sel:[1,0,0] op_sel_hi:[1,0,0]\n\t"
      "v_cvt_pk_bf16_f32 v84, v144, v145\n\t"
      "v_cvt_pk_bf16_f32 v85, v146, v147\n\t"
      "v_fma_mix_f32 v150, v142, v131, v135 op_sel:[1,0,0] op_sel_hi:[1,0,0]\n\t"
      "v_mfma_f32_16x16x32_bf16 v[200:203], a[92:95], v[24:27], v[200:203]\n\t"
      "ds_read_b128 a[48:51], v156 offset:24576\n\t"
      "v_fma_mix_f32 v151, v143, v133, v135 op_sel:[1,0,0] op_sel_hi:[1,0,0]\n\t"
      "v_cvt_pk_bf16_f32 v86, v148, v149\n\t"
      "v_mfma_f32_16x16x32_bf16 v[204:207], a[92:95], v[28:31], v[204:207]\n\t"
      "ds_read_b128 a[52:55], v156 offset:26624\n\t"
      "v_cvt_pk_bf16_f32 v87, v150, v151\n\t"
      "v_lshrrev_b32 v139, 8, v96\n\t"
      "v_lshrrev_b32 v143, 8, v97\n\t"
      "v_mfma_f32_16x16x32_bf16 v[208:211], a[96:99], v[24:27], v[208:211]\n\t"
      "buffer_load_dwordx4 v7, s[20:23], s46 offen offset:2048 lds\n\t"
      "ds_read_b128 a[56:59], v156 offset:28672\n\t"
      "v_and_b32 v136, v11, v96\n\t"
      "v_and_b32 v137, v12, v96\n\t"
      "v_mfma_f32_16x16x32_bf16 v[212:215], a[96:99], v[28:31], v[212:215]\n\t"
      "buffer_load_dword v119, v15, s[4:7], s79 offen\n\t"
      "v_and_b32 v140, v11, v97\n\t"
      "v_and_b32 v141, v12, v97\n\t"
      "v_mfma_f32_16x16x32_bf16 v[216:219], a[100:103], v[24:27], v[216:219]\n\t"
      "v_and_b32 v138, v11, v139\n\t"
      "v_and_b32 v139, v12, v139\n\t"
      "v_and_b32 v142, v11, v143\n\t"
      "v_and_b32 v143, v12, v143\n\t"
      "v_mfma_f32_16x16x32_bf16 v[220:223], a[100:103], v[28:31], v[220:223]\n\t"
      "v_mul_f32 v124, 0x4b800000, v120\n\t"
      "v_mul_f32 v126, 0x49800000, v120\n\t"
      "v_mul_f32_e64 v128, -v122, v120\n\t"
      "v_mul_f32 v125, 0x4b800000, v121\n\t"
      "v_mfma_f32_16x16x32_bf16 v[224:227], a[104:107], v[24:27], v[224:227]\n\t"
      "v_mul_f32 v127, 0x49800000, v121\n\t"
      "v_mul_f32_e64 v129, -v123, v121\n\t"
      "v_fma_mix_f32 v144, v136, v124, v128 op_sel_hi:[1,0,0]\n\t"
      "v_mfma_f32_16x16x32_bf16 v[228:231], a[104:107], v[28:31], v[228:231]\n\t"
      "buffer_load_dwordx4 v8, s[20:23], s46 offen offset:3072 lds\n\t"
      "v_fma_mix_f32 v145, v137, v126, v128 op_sel_hi:[1,0,0]\n\t"
      "v_fma_mix_f32 v146, v138, v124, v128 op_sel_hi:[1,0,0]\n\t"
      "v_fma_mix_f32 v147, v139, v126, v128 op_sel_hi:[1,0,0]\n\t"
      "v_mfma_f32_16x16x32_bf16 v[232:235], a[108:111], v[24:27], v[232:235]\n\t"
      "v_fma_mix_f32 v148, v136, v124, v128 op_sel:[1,0,0] op_sel_hi:[1,0,0]\n\t"
      "v_fma_mix_f32 v149, v137, v126, v128 op_sel:[1,0,0] op_sel_hi:[1,0,0]\n\t"
      "v_cvt_pk_bf16_f32 v16, v144, v145\n\t"
      "v_cvt_pk_bf16_f32 v17, v146, v147\n\t"
      "v_mfma_f32_16x16x32_bf16 v[236:239], a[108:111], v[28:31], v[236:239]\n\t"
      "v_fma_mix_f32 v150, v138, v124, v128 op_sel:[1,0,0] op_sel_hi:[1,0,0]\n\t"
      "v_fma_mix_f32 v151, v139, v126, v128 op_sel:[1,0,0] op_sel_hi:[1,0,0]\n\t"
      "v_cvt_pk_bf16_f32 v18, v148, v149\n\t"
      "v_mfma_f32_16x16x32_bf16 v[240:243], a[112:115], v[24:27], v[240:243]\n\t"
      "v_cvt_pk_bf16_f32 v19, v150, v151\n\t"
      "v_fma_mix_f32 v144, v140, v125, v129 op_sel_hi:[1,0,0]\n\t"
      "v_fma_mix_f32 v145, v141, v127, v129 op_sel_hi:[1,0,0]\n\t"
      "v_fma_mix_f32 v146, v142, v125, v129 op_sel_hi:[1,0,0]\n\t"
      "v_mfma_f32_16x16x32_bf16 v[244:247], a[112:115], v[28:31], v[244:247]\n\t"
      "v_fma_mix_f32 v147, v143, v127, v129 op_sel_hi:[1,0,0]\n\t"
      "v_fma_mix_f32 v148, v140, v125, v129 op_sel:[1,0,0] op_sel_hi:[1,0,0]\n\t"
      "v_fma_mix_f32 v149, v141, v127, v129 op_sel:[1,0,0] op_sel_hi:[1,0,0]\n\t"
      "v_cvt_pk_bf16_f32 v20, v144, v145\n\t"
      "v_mfma_f32_16x16x32_bf16 v[248:251], a[116:119], v[24:27], v[248:251]\n\t"
      "v_cvt_pk_bf16_f32 v21, v146, v147\n\t"
      "v_fma_mix_f32 v150, v142, v125, v129 op_sel:[1,0,0] op_sel_hi:[1,0,0]\n\t"
      "v_fma_mix_f32 v151, v143, v127, v129 op_sel:[1,0,0] op_sel_hi:[1,0,0]\n\t"
      "v_cvt_pk_bf16_f32 v22, v148, v149\n\t"
      "v_mfma_f32_16x16x32_bf16 v[252:255], a[116:119], v[28:31], v[252:255]\n\t"
      "v_cvt_pk_bf16_f32 v23, v150, v151\n\t"
      "s_waitcnt lgkmcnt(0)\n\t"
      "s_nop 0\n\t"
      "v_mfma_f32_16x16x32_bf16 a[120:123], a[0:3], v[16:19], a[120:123]\n\t"
      "ds_read_b128 a[60:63], v157 offset:0\n\t"
      "v_lshrrev_b32 v139, 8, v94\n\t"
      "v_lshrrev_b32 v143, 8, v95\n\t"
      "v_mfma_f32_16x16x32_bf16 a[124:127], a[0:3], v[20:23], a[124:127]\n\t"
      "s_add_u32 m0, s19, 65536\n\t"
      "ds_read_b128 a[64:67], v157 offset:2048\n\t"
      "v_and_b32_e64 v136, v11, v94\n\t"
      "v_mfma_f32_16x16x32_bf16 a[128:131], a[4:7], v[16:19], a[128:131]\n\t"
      "buffer_load_dwordx4 v1, s[20:23], s47 offen offset:0 lds\n\t"
      "ds_read_b128 a[68:71], v157 offset:4096\n\t"
      "v_and_b32_e64 v137, v12, v94\n\t"
      "v_mfma_f32_16x16x32_bf16 a[132:135], a[4:7], v[20:23], a[132:135]\n\t"
      "buffer_load_dwordx2 v[106:107], v14, s[24:27], s62 offen\n\t"
      "v_and_b32 v140, v11, v95\n\t"
      "v_and_b32 v141, v12, v95\n\t"
      "v_and_b32_e64 v138, v11, v139\n\t"
      "v_mfma_f32_16x16x32_bf16 a[136:139], a[8:11], v[16:19], a[136:139]\n\t"
      "ds_read_b128 a[72:75], v157 offset:6144\n\t"
      "v_and_b32 v139, v12, v139\n\t"
      "v_and_b32 v142, v11, v143\n\t"
      "v_mfma_f32_16x16x32_bf16 a[140:143], a[8:11], v[20:23], a[140:143]\n\t"
      "ds_read_b128 a[76:79], v157 offset:8192\n\t"
      "v_and_b32_e64 v143, v12, v143\n\t"
      "v_fma_mix_f32 v144, v136, v130, v134 op_sel_hi:[1,0,0]\n\t"
      "v_mfma_f32_16x16x32_bf16 v[160:163], a[12:15], v[16:19], v[160:163]\n\t"
      "ds_read_b128 a[80:83], v157 offset:10240\n\t"
      "v_fma_mix_f32 v145, v137, v132, v134 op_sel_hi:[1,0,0]\n\t"
      "v_fma_mix_f32 v146, v138, v130, v134 op_sel_hi:[1,0,0]\n\t"
      "v_mfma_f32_16x16x32_bf16 v[164:167], a[12:15], v[20:23], v[164:167]\n\t"
      "buffer_load_dwordx4 v2, s[20:23], s47 offen offset:1024 lds\n\t"
      "v_fma_mix_f32 v147, v139, v132, v134 op_sel_hi:[1,0,0]\n\t"
      "v_fma_mix_f32 v148, v136, v130, v134 op_sel:[1,0,0] op_sel_hi:[1,0,0]\n\t"
      "v_fma_mix_f32 v149, v137, v132, v134 op_sel:[1,0,0] op_sel_hi:[1,0,0]\n\t"
      "v_mfma_f32_16x16x32_bf16 v[168:171], a[16:19], v[16:19], v[168:171]\n\t"
      "ds_read_b128 a[84:87], v157 offset:12288\n\t"
      "v_cvt_pk_bf16_f32 v88, v144, v145\n\t"
      "v_cvt_pk_bf16_f32 v89, v146, v147\n\t"
      "v_mfma_f32_16x16x32_bf16 v[172:175], a[16:19], v[20:23], v[172:175]\n\t"
      "ds_read_b128 a[88:91], v157 offset:14336\n\t"
      "v_fma_mix_f32 v150, v138, v130, v134 op_sel:[1,0,0] op_sel_hi:[1,0,0]\n\t"
      "v_fma_mix_f32 v151, v139, v132, v134 op_sel:[1,0,0] op_sel_hi:[1,0,0]\n\t"
      "v_mfma_f32_16x16x32_bf16 v[176:179], a[20:23], v[16:19], v[176:179]\n\t"
      "ds_read_b128 a[92:95], v157 offset:16384\n\t"
      "v_cvt_pk_bf16_f32 v90, v148, v149\n\t"
      "v_cvt_pk_bf16_f32 v91, v150, v151\n\t"
      "v_fma_mix_f32 v144, v140, v131, v135 op_sel_hi:[1,0,0]\n\t"
      "v_mfma_f32_16x16x32_bf16 v[180:183], a[20:23], v[20:23], v[180:183]\n\t"
      "v_fma_mix_f32 v145, v141, v133, v135 op_sel_hi:[1,0,0]\n\t"
      "v_fma_mix_f32 v146, v142, v131, v135 op_sel_hi:[1,0,0]\n\t"
      "v_fma_mix_f32 v147, v143, v133, v135 op_sel_hi:[1,0,0]\n\t"
      "v_mfma_f32_16x16x32_bf16 v[184:187], a[24:27], v[16:19], v[184:187]\n\t"
      "buffer_load_dwordx4 v3, s[20:23], s47 offen offset:2048 lds\n\t"
      "ds_read_b128 a[96:99], v157 offset:18432\n\t"
      "v_fma_mix_f32 v148, v140, v131, v135 op_sel:[1,0,0] op_sel_hi:[1,0,0]\n\t"
      "v_mfma_f32_16x16x32_bf16 v[188:191], a[24:27], v[20:23], v[188:191]\n\t"
      "ds_read_b128 a[100:103], v157 offset:20480\n\t"
      "v_fma_mix_f32 v149, v141, v133, v135 op_sel:[1,0,0] op_sel_hi:[1,0,0]\n\t"
      "v_cvt_pk_bf16_f32 v92, v144, v145\n\t"
      "v_mfma_f32_16x16x32_bf16 v[192:195], a[28:31], v[16:19], v[192:195]\n\t"
      "ds_read_b128 a[104:107], v157 offset:22528\n\t"
      "v_cvt_pk_bf16_f32 v93, v146, v147\n\t"
      "v_fma_mix_f32 v150, v142, v131, v135 op_sel:[1,0,0] op_sel_hi:[1,0,0]\n\t"
      "v_fma_mix_f32 v151, v143, v133, v135 op_sel:[1,0,0] op_sel_hi:[1,0,0]\n\t"
      "v_mfma_f32_16x16x32_bf16 v[196:199], a[28:31], v[20:23], v[196:199]\n\t"
      "v_cvt_pk_bf16_f32 v94, v148, v149\n\t"
      "v_cvt_pk_bf16_f32 v95, v150, v151\n\t"
      "v_lshrrev_b32_e64 v139, 8, v98\n\t"
      "v_mfma_f32_16x16x32_bf16 v[200:203], a[32:35], v[16:19], v[200:203]\n\t"
      "ds_read_b128 a[108:111], v157 offset:24576\n\t"
      "v_lshrrev_b32 v143, 8, v99\n\t"
      "v_and_b32 v136, v11, v98\n\t"
      "v_mfma_f32_16x16x32_bf16 v[204:207], a[32:35], v[20:23], v[204:207]\n\t"
      "ds_read_b128 a[112:115], v157 offset:26624\n\t"
      "v_and_b32 v137, v12, v98\n\t"
      "v_and_b32 v140, v11, v99\n\t"
      "v_mfma_f32_16x16x32_bf16 v[208:211], a[36:39], v[16:19], v[208:211]\n\t"
      "buffer_load_dwordx4 v4, s[20:23], s47 offen offset:3072 lds\n\t"
      "ds_read_b128 a[116:119], v157 offset:28672\n\t"
      "v_and_b32 v141, v12, v99\n\t"
      "v_and_b32 v138, v11, v139\n\t"
      "v_mfma_f32_16x16x32_bf16 v[212:215], a[36:39], v[20:23], v[212:215]\n\t"
      "v_and_b32 v139, v12, v139\n\t"
      "v_and_b32 v142, v11, v143\n\t"
      "v_and_b32_e64 v143, v12, v143\n\t"
      "v_mfma_f32_16x16x32_bf16 v[216:219], a[40:43], v[16:19], v[216:219]\n\t"
      "v_lshlrev_b32 v120, 16, v114\n\t"
      "v_and_b32 v121, v10, v114\n\t"
      "v_lshlrev_b32_e64 v122, 16, v115\n\t"
      "v_mfma_f32_16x16x32_bf16 v[220:223], a[40:43], v[20:23], v[220:223]\n\t"
      "v_and_b32_e64 v123, v10, v115\n\t"
      "v_fma_mix_f32 v144, v136, v124, v128 op_sel_hi:[1,0,0]\n\t"
      "v_fma_mix_f32 v145, v137, v126, v128 op_sel_hi:[1,0,0]\n\t"
      "v_fma_mix_f32 v146, v138, v124, v128 op_sel_hi:[1,0,0]\n\t"
      "v_mfma_f32_16x16x32_bf16 v[224:227], a[44:47], v[16:19], v[224:227]\n\t"
      "s_add_u32 m0, s19, 69632\n\t"
      "v_fma_mix_f32 v147, v139, v126, v128 op_sel_hi:[1,0,0]\n\t"
      "v_fma_mix_f32 v148, v136, v124, v128 op_sel:[1,0,0] op_sel_hi:[1,0,0]\n\t"
      "v_mfma_f32_16x16x32_bf16 v[228:231], a[44:47], v[20:23], v[228:231]\n\t"
      "buffer_load_dwordx4 v5, s[20:23], s47 offen offset:0 lds\n\t"
      "v_fma_mix_f32 v149, v137, v126, v128 op_sel:[1,0,0] op_sel_hi:[1,0,0]\n\t"
      "v_cvt_pk_bf16_f32 v24, v144, v145\n\t"
      "v_mfma_f32_16x16x32_bf16 v[232:235], a[48:51], v[16:19], v[232:235]\n\t"
      "v_cvt_pk_bf16_f32 v25, v146, v147\n\t"
      "v_fma_mix_f32 v150, v138, v124, v128 op_sel:[1,0,0] op_sel_hi:[1,0,0]\n\t"
      "v_fma_mix_f32 v151, v139, v126, v128 op_sel:[1,0,0] op_sel_hi:[1,0,0]\n\t"
      "v_mfma_f32_16x16x32_bf16 v[236:239], a[48:51], v[20:23], v[236:239]\n\t"
      "v_cvt_pk_bf16_f32 v26, v148, v149\n\t"
      "v_cvt_pk_bf16_f32 v27, v150, v151\n\t"
      "v_fma_mix_f32 v144, v140, v125, v129 op_sel_hi:[1,0,0]\n\t"
      "v_fma_mix_f32 v145, v141, v127, v129 op_sel_hi:[1,0,0]\n\t"
      "v_mfma_f32_16x16x32_bf16 v[240:243], a[52:55], v[16:19], v[240:243]\n\t"
      "v_fma_mix_f32 v146, v142, v125, v129 op_sel_hi:[1,0,0]\n\t"
      "v_fma_mix_f32 v147, v143, v127, v129 op_sel_hi:[1,0,0]\n\t"
      "v_fma_mix_f32 v148, v140, v125, v129 op_sel:[1,0,0] op_sel_hi:[1,0,0]\n\t"
      "v_mfma_f32_16x16x32_bf16 v[244:247], a[52:55], v[20:23], v[244:247]\n\t"
      "v_fma_mix_f32 v149, v141, v127, v129 op_sel:[1,0,0] op_sel_hi:[1,0,0]\n\t"
      "v_cvt_pk_bf16_f32 v28, v144, v145\n\t"
      "v_cvt_pk_bf16_f32 v29, v146, v147\n\t"
      "v_mfma_f32_16x16x32_bf16 v[248:251], a[56:59], v[16:19], v[248:251]\n\t"
      "v_fma_mix_f32 v150, v142, v125, v129 op_sel:[1,0,0] op_sel_hi:[1,0,0]\n\t"
      "v_fma_mix_f32 v151, v143, v127, v129 op_sel:[1,0,0] op_sel_hi:[1,0,0]\n\t"
      "v_cvt_pk_bf16_f32 v30, v148, v149\n\t"
      "v_cvt_pk_bf16_f32 v31, v150, v151\n\t"
      "v_mfma_f32_16x16x32_bf16 v[252:255], a[56:59], v[20:23], v[252:255]\n\t"
      "s_waitcnt vmcnt(17) lgkmcnt(0)\n\t"
      "s_barrier\n\t"
      "v_mfma_f32_16x16x32_bf16 a[120:123], a[60:63], v[24:27], a[120:123]\n\t"
      "ds_read_b128 a[0:3], v152 offset:0\n\t"
      "v_lshrrev_b32_e64 v139, 8, v100\n\t"
      "v_mfma_f32_16x16x32_bf16 a[124:127], a[60:63], v[28:31], a[124:127]\n\t"
      "ds_read_b128 a[4:7], v152 offset:2048\n\t"
      "v_lshrrev_b32_e64 v143, 8, v101\n\t"
      "v_mfma_f32_16x16x32_bf16 a[128:131], a[64:67], v[24:27], a[128:131]\n\t"
      "buffer_load_dwordx4 v6, s[20:23], s47 offen offset:1024 lds\n\t"
      "ds_read_b128 a[8:11], v152 offset:4096\n\t"
      "v_mfma_f32_16x16x32_bf16 a[132:135], a[64:67], v[28:31], a[132:135]\n\t"
      "buffer_load_dwordx2 v[108:109], v13, s[24:27], s63 offen\n\t"
      "v_and_b32_e64 v136, v11, v100\n\t"
      "v_mfma_f32_16x16x32_bf16 a[136:139], a[68:71], v[24:27], a[136:139]\n\t"
      "ds_read_b128 a[12:15], v152 offset:6144\n\t"
      "v_and_b32_e64 v137, v12, v100\n\t"
      "v_mfma_f32_16x16x32_bf16 a[140:143], a[68:71], v[28:31], a[140:143]\n\t"
      "ds_read_b128 a[16:19], v152 offset:8192\n\t"
      "v_and_b32 v140, v11, v101\n\t"
      "v_and_b32 v141, v12, v101\n\t"
      "v_mfma_f32_16x16x32_bf16 v[160:163], a[72:75], v[24:27], v[160:163]\n\t"
      "ds_read_b128 a[20:23], v152 offset:10240\n\t"
      "v_and_b32_e64 v138, v11, v139\n\t"
      "v_mfma_f32_16x16x32_bf16 v[164:167], a[72:75], v[28:31], v[164:167]\n\t"
      "buffer_load_dwordx4 v7, s[20:23], s47 offen offset:2048 lds\n\t"
      "v_and_b32_e64 v139, v12, v139\n\t"
      "v_mfma_f32_16x16x32_bf16 v[168:171], a[76:79], v[24:27], v[168:171]\n\t"
      "ds_read_b128 a[24:27], v152 offset:12288\n\t"
      "v_and_b32_e64 v142, v11, v143\n\t"
      "v_mfma_f32_16x16x32_bf16 v[172:175], a[76:79], v[28:31], v[172:175]\n\t"
      "ds_read_b128 a[28:31], v152 offset:14336\n\t"
      "v_and_b32_e64 v143, v12, v143\n\t"
      "v_mfma_f32_16x16x32_bf16 v[176:179], a[80:83], v[24:27], v[176:179]\n\t"
      "ds_read_b128 a[32:35], v152 offset:16384\n\t"
      "v_mul_f32 v124, 0x4b800000, v120\n\t"
      "v_mfma_f32_16x16x32_bf16 v[180:183], a[80:83], v[28:31], v[180:183]\n\t"
      "buffer_load_dword v112, v15, s[28:31], s80 offen\n\t"
      "v_mul_f32 v126, 0x49800000, v120\n\t"
      "v_mul_f32_e64 v128, -v122, v120\n\t"
      "v_mfma_f32_16x16x32_bf16 v[184:187], a[84:87], v[24:27], v[184:187]\n\t"
      "buffer_load_dwordx4 v8, s[20:23], s47 offen offset:3072 lds\n\t"
      "ds_read_b128 a[36:39], v152 offset:18432\n\t"
      "v_mfma_f32_16x16x32_bf16 v[188:191], a[84:87], v[28:31], v[188:191]\n\t"
      "ds_read_b128 a[40:43], v152 offset:20480\n\t"
      "v_mul_f32 v125, 0x4b800000, v121\n\t"
      "v_mfma_f32_16x16x32_bf16 v[192:195], a[88:91], v[24:27], v[192:195]\n\t"
      "ds_read_b128 a[44:47], v152 offset:22528\n\t"
      "v_mul_f32 v127, 0x49800000, v121\n\t"
      "v_mfma_f32_16x16x32_bf16 v[196:199], a[88:91], v[28:31], v[196:199]\n\t"
      "v_mul_f32_e64 v129, -v123, v121\n\t"
      "v_fma_mix_f32 v144, v136, v124, v128 op_sel_hi:[1,0,0]\n\t"
      "v_mfma_f32_16x16x32_bf16 v[200:203], a[92:95], v[24:27], v[200:203]\n\t"
      "ds_read_b128 a[48:51], v152 offset:24576\n\t"
      "v_fma_mix_f32 v145, v137, v126, v128 op_sel_hi:[1,0,0]\n\t"
      "v_mfma_f32_16x16x32_bf16 v[204:207], a[92:95], v[28:31], v[204:207]\n\t"
      "s_add_u32 m0, s19, 98304\n\t"
      "ds_read_b128 a[52:55], v152 offset:26624\n\t"
      "v_fma_mix_f32 v146, v138, v124, v128 op_sel_hi:[1,0,0]\n\t"
      "v_mfma_f32_16x16x32_bf16 v[208:211], a[96:99], v[24:27], v[208:211]\n\t"
      "buffer_load_dwordx4 v1, s[20:23], s48 offen offset:0 lds\n\t"
      "ds_read_b128 a[56:59], v152 offset:28672\n\t"
      "v_mfma_f32_16x16x32_bf16 v[212:215], a[96:99], v[28:31], v[212:215]\n\t"
      "buffer_load_dword v113, v15, s[4:7], s80 offen\n\t"
      "v_fma_mix_f32 v147, v139, v126, v128 op_sel_hi:[1,0,0]\n\t"
      "v_mfma_f32_16x16x32_bf16 v[216:219], a[100:103], v[24:27], v[216:219]\n\t"
      "v_fma_mix_f32 v148, v136, v124, v128 op_sel:[1,0,0] op_sel_hi:[1,0,0]\n\t"
      "v_fma_mix_f32 v149, v137, v126, v128 op_sel:[1,0,0] op_sel_hi:[1,0,0]\n\t"
      "v_mfma_f32_16x16x32_bf16 v[220:223], a[100:103], v[28:31], v[220:223]\n\t"
      "v_cvt_pk_bf16_f32 v16, v144, v145\n\t"
      "v_cvt_pk_bf16_f32 v17, v146, v147\n\t"
      "v_mfma_f32_16x16x32_bf16 v[224:227], a[104:107], v[24:27], v[224:227]\n\t"
      "v_fma_mix_f32 v150, v138, v124, v128 op_sel:[1,0,0] op_sel_hi:[1,0,0]\n\t"
      "v_fma_mix_f32 v151, v139, v126, v128 op_sel:[1,0,0] op_sel_hi:[1,0,0]\n\t"
      "v_mfma_f32_16x16x32_bf16 v[228:231], a[104:107], v[28:31], v[228:231]\n\t"
      "buffer_load_dwordx4 v2, s[20:23], s48 offen offset:1024 lds\n\t"
      "v_cvt_pk_bf16_f32 v18, v148, v149\n\t"
      "v_cvt_pk_bf16_f32 v19, v150, v151\n\t"
      "v_mfma_f32_16x16x32_bf16 v[232:235], a[108:111], v[24:27], v[232:235]\n\t"
      "v_fma_mix_f32 v144, v140, v125, v129 op_sel_hi:[1,0,0]\n\t"
      "v_fma_mix_f32 v145, v141, v127, v129 op_sel_hi:[1,0,0]\n\t"
      "v_mfma_f32_16x16x32_bf16 v[236:239], a[108:111], v[28:31], v[236:239]\n\t"
      "v_fma_mix_f32 v146, v142, v125, v129 op_sel_hi:[1,0,0]\n\t"
      "v_fma_mix_f32 v147, v143, v127, v129 op_sel_hi:[1,0,0]\n\t"
      "v_mfma_f32_16x16x32_bf16 v[240:243], a[112:115], v[24:27], v[240:243]\n\t"
      "v_fma_mix_f32 v148, v140, v125, v129 op_sel:[1,0,0] op_sel_hi:[1,0,0]\n\t"
      "v_fma_mix_f32 v149, v141, v127, v129 op_sel:[1,0,0] op_sel_hi:[1,0,0]\n\t"
      "v_mfma_f32_16x16x32_bf16 v[244:247], a[112:115], v[28:31], v[244:247]\n\t"
      "v_cvt_pk_bf16_f32 v20, v144, v145\n\t"
      "v_cvt_pk_bf16_f32 v21, v146, v147\n\t"
      "v_mfma_f32_16x16x32_bf16 v[248:251], a[116:119], v[24:27], v[248:251]\n\t"
      "v_fma_mix_f32 v150, v142, v125, v129 op_sel:[1,0,0] op_sel_hi:[1,0,0]\n\t"
      "v_fma_mix_f32 v151, v143, v127, v129 op_sel:[1,0,0] op_sel_hi:[1,0,0]\n\t"
      "v_cvt_pk_bf16_f32 v22, v148, v149\n\t"
      "v_mfma_f32_16x16x32_bf16 v[252:255], a[116:119], v[28:31], v[252:255]\n\t"
      "v_cvt_pk_bf16_f32 v23, v150, v151\n\t"
      "s_waitcnt lgkmcnt(0)\n\t"
      "s_nop 0\n\t"
      "v_mfma_f32_16x16x32_bf16 a[120:123], a[0:3], v[16:19], a[120:123]\n\t"
      "ds_read_b128 a[60:63], v153 offset:0\n\t"
      "v_lshrrev_b32_e64 v139, 8, v102\n\t"
      "v_mfma_f32_16x16x32_bf16 a[124:127], a[0:3], v[20:23], a[124:127]\n\t"
      "ds_read_b128 a[64:67], v153 offset:2048\n\t"
      "v_lshrrev_b32_e64 v143, 8, v103\n\t"
      "v_mfma_f32_16x16x32_bf16 a[128:131], a[4:7], v[16:19], a[128:131]\n\t"
      "buffer_load_dwordx4 v3, s[20:23], s48 offen offset:2048 lds\n\t"
      "ds_read_b128 a[68:71], v153 offset:4096\n\t"
      "v_mfma_f32_16x16x32_bf16 a[132:135], a[4:7], v[20:23], a[132:135]\n\t"
      "buffer_load_dwordx2 v[110:111], v14, s[24:27], s63 offen\n\t"
      "v_and_b32_e64 v136, v11, v102\n\t"
      "v_mfma_f32_16x16x32_bf16 a[136:139], a[8:11], v[16:19], a[136:139]\n\t"
      "ds_read_b128 a[72:75], v153 offset:6144\n\t"
      "v_and_b32_e64 v137, v12, v102\n\t"
      "v_mfma_f32_16x16x32_bf16 a[140:143], a[8:11], v[20:23], a[140:143]\n\t"
      "ds_read_b128 a[76:79], v153 offset:8192\n\t"
      "v_and_b32_e64 v140, v11, v103\n\t"
      "v_mfma_f32_16x16x32_bf16 v[160:163], a[12:15], v[16:19], v[160:163]\n\t"
      "ds_read_b128 a[80:83], v153 offset:10240\n\t"
      "v_and_b32_e64 v141, v12, v103\n\t"
      "v_mfma_f32_16x16x32_bf16 v[164:167], a[12:15], v[20:23], v[164:167]\n\t"
      "buffer_load_dwordx4 v4, s[20:23], s48 offen offset:3072 lds\n\t"
      "v_and_b32_e64 v138, v11, v139\n\t"
      "v_mfma_f32_16x16x32_bf16 v[168:171], a[16:19], v[16:19], v[168:171]\n\t"
      "ds_read_b128 a[84:87], v153 offset:12288\n\t"
      "v_and_b32_e64 v139, v12, v139\n\t"
      "v_mfma_f32_16x16x32_bf16 v[172:175], a[16:19], v[20:23], v[172:175]\n\t"
      "ds_read_b128 a[88:91], v153 offset:14336\n\t"
      "v_and_b32_e64 v142, v11, v143\n\t"
      "v_mfma_f32_16x16x32_bf16 v[176:179], a[20:23], v[16:19], v[176:179]\n\t"
      "ds_read_b128 a[92:95], v153 offset:16384\n\t"
      "v_and_b32_e64 v143, v12, v143\n\t"
      "v_mfma_f32_16x16x32_bf16 v[180:183], a[20:23], v[20:23], v[180:183]\n\t"
      "s_add_u32 m0, s19, 102400\n\t"
      "v_lshlrev_b32_e64 v120, 16, v116\n\t"
      "v_mfma_f32_16x16x32_bf16 v[184:187], a[24:27], v[16:19], v[184:187]\n\t"
      "buffer_load_dwordx4 v5, s[20:23], s48 offen offset:0 lds\n\t"
      "ds_read_b128 a[96:99], v153 offset:18432\n\t"
      "v_mfma_f32_16x16x32_bf16 v[188:191], a[24:27], v[20:23], v[188:191]\n\t"
      "ds_read_b128 a[100:103], v153 offset:20480\n\t"
      "v_and_b32_e64 v121, v10, v116\n\t"
      "v_mfma_f32_16x16x32_bf16 v[192:195], a[28:31], v[16:19], v[192:195]\n\t"
      "ds_read_b128 a[104:107], v153 offset:22528\n\t"
      "v_lshlrev_b32 v122, 16, v117\n\t"
      "v_and_b32 v123, v10, v117\n\t"
      "v_mfma_f32_16x16x32_bf16 v[196:199], a[28:31], v[20:23], v[196:199]\n\t"
      "v_fma_mix_f32 v144, v136, v124, v128 op_sel_hi:[1,0,0]\n\t"
      "v_fma_mix_f32 v145, v137, v126, v128 op_sel_hi:[1,0,0]\n\t"
      "v_mfma_f32_16x16x32_bf16 v[200:203], a[32:35], v[16:19], v[200:203]\n\t"
      "ds_read_b128 a[108:111], v153 offset:24576\n\t"
      "v_fma_mix_f32 v146, v138, v124, v128 op_sel_hi:[1,0,0]\n\t"
      "v_mfma_f32_16x16x32_bf16 v[204:207], a[32:35], v[20:23], v[204:207]\n\t"
      "ds_read_b128 a[112:115], v153 offset:26624\n\t"
      "v_fma_mix_f32 v147, v139, v126, v128 op_sel_hi:[1,0,0]\n\t"
      "v_mfma_f32_16x16x32_bf16 v[208:211], a[36:39], v[16:19], v[208:211]\n\t"
      "buffer_load_dwordx4 v6, s[20:23], s48 offen offset:1024 lds\n\t"
      "ds_read_b128 a[116:119], v153 offset:28672\n\t"
      "v_mfma_f32_16x16x32_bf16 v[212:215], a[36:39], v[20:23], v[212:215]\n\t"
      "v_fma_mix_f32 v148, v136, v124, v128 op_sel:[1,0,0] op_sel_hi:[1,0,0]\n\t"
      "v_fma_mix_f32 v149, v137, v126, v128 op_sel:[1,0,0] op_sel_hi:[1,0,0]\n\t"
      "v_mfma_f32_16x16x32_bf16 v[216:219], a[40:43], v[16:19], v[216:219]\n\t"
      "v_cvt_pk_bf16_f32 v24, v144, v145\n\t"
      "v_cvt_pk_bf16_f32 v25, v146, v147\n\t"
      "v_mfma_f32_16x16x32_bf16 v[220:223], a[40:43], v[20:23], v[220:223]\n\t"
      "v_fma_mix_f32 v150, v138, v124, v128 op_sel:[1,0,0] op_sel_hi:[1,0,0]\n\t"
      "v_fma_mix_f32 v151, v139, v126, v128 op_sel:[1,0,0] op_sel_hi:[1,0,0]\n\t"
      "v_mfma_f32_16x16x32_bf16 v[224:227], a[44:47], v[16:19], v[224:227]\n\t"
      "v_cvt_pk_bf16_f32 v26, v148, v149\n\t"
      "v_cvt_pk_bf16_f32 v27, v150, v151\n\t"
      "v_mfma_f32_16x16x32_bf16 v[228:231], a[44:47], v[20:23], v[228:231]\n\t"
      "buffer_load_dwordx4 v7, s[20:23], s48 offen offset:2048 lds\n\t"
      "v_fma_mix_f32 v144, v140, v125, v129 op_sel_hi:[1,0,0]\n\t"
      "v_mfma_f32_16x16x32_bf16 v[232:235], a[48:51], v[16:19], v[232:235]\n\t"
      "v_fma_mix_f32 v145, v141, v127, v129 op_sel_hi:[1,0,0]\n\t"
      "v_fma_mix_f32 v146, v142, v125, v129 op_sel_hi:[1,0,0]\n\t"
      "v_mfma_f32_16x16x32_bf16 v[236:239], a[48:51], v[20:23], v[236:239]\n\t"
      "v_fma_mix_f32 v147, v143, v127, v129 op_sel_hi:[1,0,0]\n\t"
      "v_fma_mix_f32 v148, v140, v125, v129 op_sel:[1,0,0] op_sel_hi:[1,0,0]\n\t"
      "v_mfma_f32_16x16x32_bf16 v[240:243], a[52:55], v[16:19], v[240:243]\n\t"
      "v_fma_mix_f32 v149, v141, v127, v129 op_sel:[1,0,0] op_sel_hi:[1,0,0]\n\t"
      "v_cvt_pk_bf16_f32 v28, v144, v145\n\t"
      "v_mfma_f32_16x16x32_bf16 v[244:247], a[52:55], v[20:23], v[244:247]\n\t"
      "v_cvt_pk_bf16_f32 v29, v146, v147\n\t"
      "v_fma_mix_f32 v150, v142, v125, v129 op_sel:[1,0,0] op_sel_hi:[1,0,0]\n\t"
      "v_mfma_f32_16x16x32_bf16 v[248:251], a[56:59], v[16:19], v[248:251]\n\t"
      "v_fma_mix_f32 v151, v143, v127, v129 op_sel:[1,0,0] op_sel_hi:[1,0,0]\n\t"
      "v_cvt_pk_bf16_f32 v30, v148, v149\n\t"
      "v_cvt_pk_bf16_f32 v31, v150, v151\n\t"
      "v_mfma_f32_16x16x32_bf16 v[252:255], a[56:59], v[20:23], v[252:255]\n\t"
      "s_waitcnt vmcnt(18) lgkmcnt(0)\n\t"
      "s_barrier\n\t"
      "v_mfma_f32_16x16x32_bf16 a[120:123], a[60:63], v[24:27], a[120:123]\n\t"
      "ds_read_b128 a[0:3], v152 offset:32768\n\t"
      "v_lshrrev_b32_e64 v139, 8, v104\n\t"
      "v_mfma_f32_16x16x32_bf16 a[124:127], a[60:63], v[28:31], a[124:127]\n\t"
      "ds_read_b128 a[4:7], v152 offset:34816\n\t"
      "v_lshrrev_b32_e64 v143, 8, v105\n\t"
      "v_mfma_f32_16x16x32_bf16 a[128:131], a[64:67], v[24:27], a[128:131]\n\t"
      "buffer_load_dwordx4 v8, s[20:23], s48 offen offset:3072 lds\n\t"
      "ds_read_b128 a[8:11], v152 offset:36864\n\t"
      "v_mfma_f32_16x16x32_bf16 a[132:135], a[64:67], v[28:31], a[132:135]\n\t"
      "buffer_load_dwordx2 v[96:97], v13, s[24:27], s64 offen\n\t"
      "v_and_b32_e64 v136, v11, v104\n\t"
      "v_mfma_f32_16x16x32_bf16 a[136:139], a[68:71], v[24:27], a[136:139]\n\t"
      "ds_read_b128 a[12:15], v152 offset:38912\n\t"
      "v_and_b32_e64 v137, v12, v104\n\t"
      "v_mfma_f32_16x16x32_bf16 a[140:143], a[68:71], v[28:31], a[140:143]\n\t"
      "ds_read_b128 a[16:19], v152 offset:40960\n\t"
      "v_and_b32 v140, v11, v105\n\t"
      "v_and_b32 v141, v12, v105\n\t"
      "v_mfma_f32_16x16x32_bf16 v[160:163], a[72:75], v[24:27], v[160:163]\n\t"
      "s_add_u32 m0, s19, 131072\n\t"
      "ds_read_b128 a[20:23], v152 offset:43008\n\t"
      "v_mfma_f32_16x16x32_bf16 v[164:167], a[72:75], v[28:31], v[164:167]\n\t"
      "buffer_load_dwordx4 v1, s[20:23], s49 offen offset:0 lds\n\t"
      "v_and_b32_e64 v138, v11, v139\n\t"
      "v_mfma_f32_16x16x32_bf16 v[168:171], a[76:79], v[24:27], v[168:171]\n\t"
      "ds_read_b128 a[24:27], v152 offset:45056\n\t"
      "v_and_b32_e64 v139, v12, v139\n\t"
      "v_mfma_f32_16x16x32_bf16 v[172:175], a[76:79], v[28:31], v[172:175]\n\t"
      "ds_read_b128 a[28:31], v152 offset:47104\n\t"
      "v_and_b32_e64 v142, v11, v143\n\t"
      "v_mfma_f32_16x16x32_bf16 v[176:179], a[80:83], v[24:27], v[176:179]\n\t"
      "ds_read_b128 a[32:35], v152 offset:49152\n\t"
      "v_and_b32_e64 v143, v12, v143\n\t"
      "v_mfma_f32_16x16x32_bf16 v[180:183], a[80:83], v[28:31], v[180:183]\n\t"
      "buffer_load_dword v114, v15, s[28:31], s81 offen\n\t"
      "v_mul_f32 v124, 0x4b800000, v120\n\t"
      "v_mul_f32 v126, 0x49800000, v120\n\t"
      "v_mfma_f32_16x16x32_bf16 v[184:187], a[84:87], v[24:27], v[184:187]\n\t"
      "buffer_load_dwordx4 v2, s[20:23], s49 offen offset:1024 lds\n\t"
      "ds_read_b128 a[36:39], v152 offset:51200\n\t"
      "v_mfma_f32_16x16x32_bf16 v[188:191], a[84:87], v[28:31], v[188:191]\n\t"
      "ds_read_b128 a[40:43], v152 offset:53248\n\t"
      "v_mul_f32_e64 v128, -v122, v120\n\t"
      "v_mfma_f32_16x16x32_bf16 v[192:195], a[88:91], v[24:27], v[192:195]\n\t"
      "ds_read_b128 a[44:47], v152 offset:55296\n\t"
      "v_mul_f32 v125, 0x4b800000, v121\n\t"
      "v_mfma_f32_16x16x32_bf16 v[196:199], a[88:91], v[28:31], v[196:199]\n\t"
      "v_mul_f32 v127, 0x49800000, v121\n\t"
      "v_mul_f32_e64 v129, -v123, v121\n\t"
      "v_mfma_f32_16x16x32_bf16 v[200:203], a[92:95], v[24:27], v[200:203]\n\t"
      "ds_read_b128 a[48:51], v152 offset:57344\n\t"
      "v_fma_mix_f32 v144, v136, v124, v128 op_sel_hi:[1,0,0]\n\t"
      "v_mfma_f32_16x16x32_bf16 v[204:207], a[92:95], v[28:31], v[204:207]\n\t"
      "ds_read_b128 a[52:55], v152 offset:59392\n\t"
      "v_fma_mix_f32 v145, v137, v126, v128 op_sel_hi:[1,0,0]\n\t"
      "v_fma_mix_f32 v146, v138, v124, v128 op_sel_hi:[1,0,0]\n\t"
      "v_mfma_f32_16x16x32_bf16 v[208:211], a[96:99], v[24:27], v[208:211]\n\t"
      "buffer_load_dwordx4 v3, s[20:23], s49 offen offset:2048 lds\n\t"
      "ds_read_b128 a[56:59], v152 offset:61440\n\t"
      "v_mfma_f32_16x16x32_bf16 v[212:215], a[96:99], v[28:31], v[212:215]\n\t"
      "buffer_load_dword v115, v15, s[4:7], s81 offen\n\t"
      "v_fma_mix_f32 v147, v139, v126, v128 op_sel_hi:[1,0,0]\n\t"
      "v_mfma_f32_16x16x32_bf16 v[216:219], a[100:103], v[24:27], v[216:219]\n\t"
      "v_fma_mix_f32 v148, v136, v124, v128 op_sel:[1,0,0] op_sel_hi:[1,0,0]\n\t"
      "v_fma_mix_f32 v149, v137, v126, v128 op_sel:[1,0,0] op_sel_hi:[1,0,0]\n\t"
      "v_mfma_f32_16x16x32_bf16 v[220:223], a[100:103], v[28:31], v[220:223]\n\t"
      "v_cvt_pk_bf16_f32 v16, v144, v145\n\t"
      "v_cvt_pk_bf16_f32 v17, v146, v147\n\t"
      "v_mfma_f32_16x16x32_bf16 v[224:227], a[104:107], v[24:27], v[224:227]\n\t"
      "v_fma_mix_f32 v150, v138, v124, v128 op_sel:[1,0,0] op_sel_hi:[1,0,0]\n\t"
      "v_fma_mix_f32 v151, v139, v126, v128 op_sel:[1,0,0] op_sel_hi:[1,0,0]\n\t"
      "v_mfma_f32_16x16x32_bf16 v[228:231], a[104:107], v[28:31], v[228:231]\n\t"
      "buffer_load_dwordx4 v4, s[20:23], s49 offen offset:3072 lds\n\t"
      "v_cvt_pk_bf16_f32 v18, v148, v149\n\t"
      "v_cvt_pk_bf16_f32 v19, v150, v151\n\t"
      "v_mfma_f32_16x16x32_bf16 v[232:235], a[108:111], v[24:27], v[232:235]\n\t"
      "v_fma_mix_f32 v144, v140, v125, v129 op_sel_hi:[1,0,0]\n\t"
      "v_fma_mix_f32 v145, v141, v127, v129 op_sel_hi:[1,0,0]\n\t"
      "v_mfma_f32_16x16x32_bf16 v[236:239], a[108:111], v[28:31], v[236:239]\n\t"
      "v_fma_mix_f32 v146, v142, v125, v129 op_sel_hi:[1,0,0]\n\t"
      "v_fma_mix_f32 v147, v143, v127, v129 op_sel_hi:[1,0,0]\n\t"
      "v_mfma_f32_16x16x32_bf16 v[240:243], a[112:115], v[24:27], v[240:243]\n\t"
      "v_fma_mix_f32 v148, v140, v125, v129 op_sel:[1,0,0] op_sel_hi:[1,0,0]\n\t"
      "v_fma_mix_f32 v149, v141, v127, v129 op_sel:[1,0,0] op_sel_hi:[1,0,0]\n\t"
      "v_mfma_f32_16x16x32_bf16 v[244:247], a[112:115], v[28:31], v[244:247]\n\t"
      "v_cvt_pk_bf16_f32 v20, v144, v145\n\t"
      "v_cvt_pk_bf16_f32 v21, v146, v147\n\t"
      "v_mfma_f32_16x16x32_bf16 v[248:251], a[116:119], v[24:27], v[248:251]\n\t"
      "v_fma_mix_f32 v150, v142, v125, v129 op_sel:[1,0,0] op_sel_hi:[1,0,0]\n\t"
      "v_fma_mix_f32 v151, v143, v127, v129 op_sel:[1,0,0] op_sel_hi:[1,0,0]\n\t"
      "v_cvt_pk_bf16_f32 v22, v148, v149\n\t"
      "v_mfma_f32_16x16x32_bf16 v[252:255], a[116:119], v[28:31], v[252:255]\n\t"
      "v_cvt_pk_bf16_f32 v23, v150, v151\n\t"
      "s_waitcnt lgkmcnt(0)\n\t"
      "s_nop 0\n\t"
      "v_mfma_f32_16x16x32_bf16 a[120:123], a[0:3], v[16:19], a[120:123]\n\t"
      "ds_read_b128 a[60:63], v153 offset:32768\n\t"
      "v_lshrrev_b32_e64 v139, 8, v106\n\t"
      "v_mfma_f32_16x16x32_bf16 a[124:127], a[0:3], v[20:23], a[124:127]\n\t"
      "s_add_u32 m0, s19, 135168\n\t"
      "ds_read_b128 a[64:67], v153 offset:34816\n\t"
      "v_mfma_f32_16x16x32_bf16 a[128:131], a[4:7], v[16:19], a[128:131]\n\t"
      "buffer_load_dwordx4 v5, s[20:23], s49 offen offset:0 lds\n\t"
      "ds_read_b128 a[68:71], v153 offset:36864\n\t"
      "v_mfma_f32_16x16x32_bf16 a[132:135], a[4:7], v[20:23], a[132:135]\n\t"
      "buffer_load_dwordx2 v[98:99], v14, s[24:27], s64 offen\n\t"
      "v_lshrrev_b32_e64 v143, 8, v107\n\t"
      "v_mfma_f32_16x16x32_bf16 a[136:139], a[8:11], v[16:19], a[136:139]\n\t"
      "ds_read_b128 a[72:75], v153 offset:38912\n\t"
      "v_and_b32_e64 v136, v11, v106\n\t"
      "v_mfma_f32_16x16x32_bf16 a[140:143], a[8:11], v[20:23], a[140:143]\n\t"
      "ds_read_b128 a[76:79], v153 offset:40960\n\t"
      "v_and_b32_e64 v137, v12, v106\n\t"
      "v_mfma_f32_16x16x32_bf16 v[160:163], a[12:15], v[16:19], v[160:163]\n\t"
      "ds_read_b128 a[80:83], v153 offset:43008\n\t"
      "v_and_b32_e64 v140, v11, v107\n\t"
      "v_mfma_f32_16x16x32_bf16 v[164:167], a[12:15], v[20:23], v[164:167]\n\t"
      "buffer_load_dwordx4 v6, s[20:23], s49 offen offset:1024 lds\n\t"
      "v_and_b32_e64 v141, v12, v107\n\t"
      "v_mfma_f32_16x16x32_bf16 v[168:171], a[16:19], v[16:19], v[168:171]\n\t"
      "ds_read_b128 a[84:87], v153 offset:45056\n\t"
      "v_and_b32_e64 v138, v11, v139\n\t"
      "v_mfma_f32_16x16x32_bf16 v[172:175], a[16:19], v[20:23], v[172:175]\n\t"
      "ds_read_b128 a[88:91], v153 offset:47104\n\t"
      "v_and_b32 v139, v12, v139\n\t"
      "v_and_b32 v142, v11, v143\n\t"
      "v_mfma_f32_16x16x32_bf16 v[176:179], a[20:23], v[16:19], v[176:179]\n\t"
      "ds_read_b128 a[92:95], v153 offset:49152\n\t"
      "v_and_b32_e64 v143, v12, v143\n\t"
      "v_mfma_f32_16x16x32_bf16 v[180:183], a[20:23], v[20:23], v[180:183]\n\t"
      "v_lshlrev_b32 v120, 16, v118\n\t"
      "v_and_b32 v121, v10, v118\n\t"
      "v_mfma_f32_16x16x32_bf16 v[184:187], a[24:27], v[16:19], v[184:187]\n\t"
      "buffer_load_dwordx4 v7, s[20:23], s49 offen offset:2048 lds\n\t"
      "ds_read_b128 a[96:99], v153 offset:51200\n\t"
      "v_mfma_f32_16x16x32_bf16 v[188:191], a[24:27], v[20:23], v[188:191]\n\t"
      "ds_read_b128 a[100:103], v153 offset:53248\n\t"
      "v_lshlrev_b32_e64 v122, 16, v119\n\t"
      "v_mfma_f32_16x16x32_bf16 v[192:195], a[28:31], v[16:19], v[192:195]\n\t"
      "ds_read_b128 a[104:107], v153 offset:55296\n\t"
      "v_and_b32_e64 v123, v10, v119\n\t"
      "v_mfma_f32_16x16x32_bf16 v[196:199], a[28:31], v[20:23], v[196:199]\n\t"
      "v_fma_mix_f32 v144, v136, v124, v128 op_sel_hi:[1,0,0]\n\t"
      "v_fma_mix_f32 v145, v137, v126, v128 op_sel_hi:[1,0,0]\n\t"
      "v_mfma_f32_16x16x32_bf16 v[200:203], a[32:35], v[16:19], v[200:203]\n\t"
      "ds_read_b128 a[108:111], v153 offset:57344\n\t"
      "v_fma_mix_f32 v146, v138, v124, v128 op_sel_hi:[1,0,0]\n\t"
      "v_mfma_f32_16x16x32_bf16 v[204:207], a[32:35], v[20:23], v[204:207]\n\t"
      "ds_read_b128 a[112:115], v153 offset:59392\n\t"
      "v_fma_mix_f32 v147, v139, v126, v128 op_sel_hi:[1,0,0]\n\t"
      "v_mfma_f32_16x16x32_bf16 v[208:211], a[36:39], v[16:19], v[208:211]\n\t"
      "buffer_load_dwordx4 v8, s[20:23], s49 offen offset:3072 lds\n\t"
      "ds_read_b128 a[116:119], v153 offset:61440\n\t"
      "v_mfma_f32_16x16x32_bf16 v[212:215], a[36:39], v[20:23], v[212:215]\n\t"
      "v_fma_mix_f32 v148, v136, v124, v128 op_sel:[1,0,0] op_sel_hi:[1,0,0]\n\t"
      "v_fma_mix_f32 v149, v137, v126, v128 op_sel:[1,0,0] op_sel_hi:[1,0,0]\n\t"
      "v_cvt_pk_bf16_f32 v24, v144, v145\n\t"
      "v_mfma_f32_16x16x32_bf16 v[216:219], a[40:43], v[16:19], v[216:219]\n\t"
      "v_cvt_pk_bf16_f32 v25, v146, v147\n\t"
      "v_fma_mix_f32 v150, v138, v124, v128 op_sel:[1,0,0] op_sel_hi:[1,0,0]\n\t"
      "v_mfma_f32_16x16x32_bf16 v[220:223], a[40:43], v[20:23], v[220:223]\n\t"
      "v_fma_mix_f32 v151, v139, v126, v128 op_sel:[1,0,0] op_sel_hi:[1,0,0]\n\t"
      "v_cvt_pk_bf16_f32 v26, v148, v149\n\t"
      "v_mfma_f32_16x16x32_bf16 v[224:227], a[44:47], v[16:19], v[224:227]\n\t"
      "s_add_u32 m0, s19, 0\n\t"
      "s_nop 0\n\t"
      "v_cvt_pk_bf16_f32 v27, v150, v151\n\t"
      "v_mfma_f32_16x16x32_bf16 v[228:231], a[44:47], v[20:23], v[228:231]\n\t"
      "buffer_load_dwordx4 v1, s[20:23], s50 offen offset:0 lds\n\t"
      "v_fma_mix_f32 v144, v140, v125, v129 op_sel_hi:[1,0,0]\n\t"
      "v_mfma_f32_16x16x32_bf16 v[232:235], a[48:51], v[16:19], v[232:235]\n\t"
      "v_fma_mix_f32 v145, v141, v127, v129 op_sel_hi:[1,0,0]\n\t"
      "v_fma_mix_f32 v146, v142, v125, v129 op_sel_hi:[1,0,0]\n\t"
      "v_mfma_f32_16x16x32_bf16 v[236:239], a[48:51], v[20:23], v[236:239]\n\t"
      "v_fma_mix_f32 v147, v143, v127, v129 op_sel_hi:[1,0,0]\n\t"
      "v_fma_mix_f32 v148, v140, v125, v129 op_sel:[1,0,0] op_sel_hi:[1,0,0]\n\t"
      "v_mfma_f32_16x16x32_bf16 v[240:243], a[52:55], v[16:19], v[240:243]\n\t"
      "v_fma_mix_f32 v149, v141, v127, v129 op_sel:[1,0,0] op_sel_hi:[1,0,0]\n\t"
      "v_cvt_pk_bf16_f32 v28, v144, v145\n\t"
      "v_mfma_f32_16x16x32_bf16 v[244:247], a[52:55], v[20:23], v[244:247]\n\t"
      "v_cvt_pk_bf16_f32 v29, v146, v147\n\t"
      "v_fma_mix_f32 v150, v142, v125, v129 op_sel:[1,0,0] op_sel_hi:[1,0,0]\n\t"
      "v_mfma_f32_16x16x32_bf16 v[248:251], a[56:59], v[16:19], v[248:251]\n\t"
      "v_fma_mix_f32 v151, v143, v127, v129 op_sel:[1,0,0] op_sel_hi:[1,0,0]\n\t"
      "v_cvt_pk_bf16_f32 v30, v148, v149\n\t"
      "v_cvt_pk_bf16_f32 v31, v150, v151\n\t"
      "v_mfma_f32_16x16x32_bf16 v[252:255], a[56:59], v[20:23], v[252:255]\n\t"
      "s_waitcnt vmcnt(18) lgkmcnt(0)\n\t"
      "s_barrier\n\t"
      "v_mfma_f32_16x16x32_bf16 a[120:123], a[60:63], v[24:27], a[120:123]\n\t"
      "ds_read_b128 a[0:3], v154 offset:0\n\t"
      "v_lshrrev_b32_e64 v139, 8, v108\n\t"
      "v_mfma_f32_16x16x32_bf16 a[124:127], a[60:63], v[28:31], a[124:127]\n\t"
      "ds_read_b128 a[4:7], v154 offset:2048\n\t"
      "v_lshrrev_b32_e64 v143, 8, v109\n\t"
      "v_mfma_f32_16x16x32_bf16 a[128:131], a[64:67], v[24:27], a[128:131]\n\t"
      "buffer_load_dwordx4 v2, s[20:23], s50 offen offset:1024 lds\n\t"
      "ds_read_b128 a[8:11], v154 offset:4096\n\t"
      "v_mfma_f32_16x16x32_bf16 a[132:135], a[64:67], v[28:31], a[132:135]\n\t"
      "buffer_load_dwordx2 v[100:101], v13, s[24:27], s65 offen\n\t"
      "v_and_b32_e64 v136, v11, v108\n\t"
      "v_mfma_f32_16x16x32_bf16 a[136:139], a[68:71], v[24:27], a[136:139]\n\t"
      "ds_read_b128 a[12:15], v154 offset:6144\n\t"
      "v_and_b32_e64 v137, v12, v108\n\t"
      "v_mfma_f32_16x16x32_bf16 a[140:143], a[68:71], v[28:31], a[140:143]\n\t"
      "ds_read_b128 a[16:19], v154 offset:8192\n\t"
      "v_and_b32 v140, v11, v109\n\t"
      "v_and_b32 v141, v12, v109\n\t"
      "v_mfma_f32_16x16x32_bf16 v[160:163], a[72:75], v[24:27], v[160:163]\n\t"
      "ds_read_b128 a[20:23], v154 offset:10240\n\t"
      "v_and_b32_e64 v138, v11, v139\n\t"
      "v_mfma_f32_16x16x32_bf16 v[164:167], a[72:75], v[28:31], v[164:167]\n\t"
      "buffer_load_dwordx4 v3, s[20:23], s50 offen offset:2048 lds\n\t"
      "v_and_b32_e64 v139, v12, v139\n\t"
      "v_mfma_f32_16x16x32_bf16 v[168:171], a[76:79], v[24:27], v[168:171]\n\t"
      "ds_read_b128 a[24:27], v154 offset:12288\n\t"
      "v_and_b32_e64 v142, v11, v143\n\t"
      "v_mfma_f32_16x16x32_bf16 v[172:175], a[76:79], v[28:31], v[172:175]\n\t"
      "ds_read_b128 a[28:31], v154 offset:14336\n\t"
      "v_and_b32_e64 v143, v12, v143\n\t"
      "v_mfma_f32_16x16x32_bf16 v[176:179], a[80:83], v[24:27], v[176:179]\n\t"
      "ds_read_b128 a[32:35], v154 offset:16384\n\t"
      "v_mul_f32 v124, 0x4b800000, v120\n\t"
      "v_mfma_f32_16x16x32_bf16 v[180:183], a[80:83], v[28:31], v[180:183]\n\t"
      "buffer_load_dword v116, v15, s[28:31], s82 offen\n\t"
      "v_mul_f32 v126, 0x49800000, v120\n\t"
      "v_mul_f32_e64 v128, -v122, v120\n\t"
      "v_mfma_f32_16x16x32_bf16 v[184:187], a[84:87], v[24:27], v[184:187]\n\t"
      "buffer_load_dwordx4 v4, s[20:23], s50 offen offset:3072 lds\n\t"
      "ds_read_b128 a[36:39], v154 offset:18432\n\t"
      "v_mfma_f32_16x16x32_bf16 v[188:191], a[84:87], v[28:31], v[188:191]\n\t"
      "ds_read_b128 a[40:43], v154 offset:20480\n\t"
      "v_mul_f32 v125, 0x4b800000, v121\n\t"
      "v_mfma_f32_16x16x32_bf16 v[192:195], a[88:91], v[24:27], v[192:195]\n\t"
      "ds_read_b128 a[44:47], v154 offset:22528\n\t"
      "v_mul_f32 v127, 0x49800000, v121\n\t"
      "v_mfma_f32_16x16x32_bf16 v[196:199], a[88:91], v[28:31], v[196:199]\n\t"
      "v_mul_f32_e64 v129, -v123, v121\n\t"
      "v_fma_mix_f32 v144, v136, v124, v128 op_sel_hi:[1,0,0]\n\t"
      "v_mfma_f32_16x16x32_bf16 v[200:203], a[92:95], v[24:27], v[200:203]\n\t"
      "ds_read_b128 a[48:51], v154 offset:24576\n\t"
      "v_fma_mix_f32 v145, v137, v126, v128 op_sel_hi:[1,0,0]\n\t"
      "v_mfma_f32_16x16x32_bf16 v[204:207], a[92:95], v[28:31], v[204:207]\n\t"
      "s_add_u32 m0, s19, 4096\n\t"
      "ds_read_b128 a[52:55], v154 offset:26624\n\t"
      "v_fma_mix_f32 v146, v138, v124, v128 op_sel_hi:[1,0,0]\n\t"
      "v_mfma_f32_16x16x32_bf16 v[208:211], a[96:99], v[24:27], v[208:211]\n\t"
      "buffer_load_dwordx4 v5, s[20:23], s50 offen offset:0 lds\n\t"
      "ds_read_b128 a[56:59], v154 offset:28672\n\t"
      "v_mfma_f32_16x16x32_bf16 v[212:215], a[96:99], v[28:31], v[212:215]\n\t"
      "buffer_load_dword v117, v15, s[4:7], s82 offen\n\t"
      "v_fma_mix_f32 v147, v139, v126, v128 op_sel_hi:[1,0,0]\n\t"
      "v_mfma_f32_16x16x32_bf16 v[216:219], a[100:103], v[24:27], v[216:219]\n\t"
      "v_fma_mix_f32 v148, v136, v124, v128 op_sel:[1,0,0] op_sel_hi:[1,0,0]\n\t"
      "v_fma_mix_f32 v149, v137, v126, v128 op_sel:[1,0,0] op_sel_hi:[1,0,0]\n\t"
      "v_mfma_f32_16x16x32_bf16 v[220:223], a[100:103], v[28:31], v[220:223]\n\t"
      "v_cvt_pk_bf16_f32 v16, v144, v145\n\t"
      "v_cvt_pk_bf16_f32 v17, v146, v147\n\t"
      "v_mfma_f32_16x16x32_bf16 v[224:227], a[104:107], v[24:27], v[224:227]\n\t"
      "v_fma_mix_f32 v150, v138, v124, v128 op_sel:[1,0,0] op_sel_hi:[1,0,0]\n\t"
      "v_fma_mix_f32 v151, v139, v126, v128 op_sel:[1,0,0] op_sel_hi:[1,0,0]\n\t"
      "v_mfma_f32_16x16x32_bf16 v[228:231], a[104:107], v[28:31], v[228:231]\n\t"
      "buffer_load_dwordx4 v6, s[20:23], s50 offen offset:1024 lds\n\t"
      "v_cvt_pk_bf16_f32 v18, v148, v149\n\t"
      "v_cvt_pk_bf16_f32 v19, v150, v151\n\t"
      "v_mfma_f32_16x16x32_bf16 v[232:235], a[108:111], v[24:27], v[232:235]\n\t"
      "v_fma_mix_f32 v144, v140, v125, v129 op_sel_hi:[1,0,0]\n\t"
      "v_fma_mix_f32 v145, v141, v127, v129 op_sel_hi:[1,0,0]\n\t"
      "v_mfma_f32_16x16x32_bf16 v[236:239], a[108:111], v[28:31], v[236:239]\n\t"
      "v_fma_mix_f32 v146, v142, v125, v129 op_sel_hi:[1,0,0]\n\t"
      "v_fma_mix_f32 v147, v143, v127, v129 op_sel_hi:[1,0,0]\n\t"
      "v_mfma_f32_16x16x32_bf16 v[240:243], a[112:115], v[24:27], v[240:243]\n\t"
      "v_fma_mix_f32 v148, v140, v125, v129 op_sel:[1,0,0] op_sel_hi:[1,0,0]\n\t"
      "v_fma_mix_f32 v149, v141, v127, v129 op_sel:[1,0,0] op_sel_hi:[1,0,0]\n\t"
      "v_mfma_f32_16x16x32_bf16 v[244:247], a[112:115], v[28:31], v[244:247]\n\t"
      "v_cvt_pk_bf16_f32 v20, v144, v145\n\t"
      "v_cvt_pk_bf16_f32 v21, v146, v147\n\t"
      "v_mfma_f32_16x16x32_bf16 v[248:251], a[116:119], v[24:27], v[248:251]\n\t"
      "v_fma_mix_f32 v150, v142, v125, v129 op_sel:[1,0,0] op_sel_hi:[1,0,0]\n\t"
      "v_fma_mix_f32 v151, v143, v127, v129 op_sel:[1,0,0] op_sel_hi:[1,0,0]\n\t"
      "v_cvt_pk_bf16_f32 v22, v148, v149\n\t"
      "v_mfma_f32_16x16x32_bf16 v[252:255], a[116:119], v[28:31], v[252:255]\n\t"
      "v_cvt_pk_bf16_f32 v23, v150, v151\n\t"
      "s_waitcnt lgkmcnt(0)\n\t"
      "s_nop 0\n\t"
      "v_mfma_f32_16x16x32_bf16 a[120:123], a[0:3], v[16:19], a[120:123]\n\t"
      "ds_read_b128 a[60:63], v155 offset:0\n\t"
      "v_lshrrev_b32_e64 v139, 8, v110\n\t"
      "v_mfma_f32_16x16x32_bf16 a[124:127], a[0:3], v[20:23], a[124:127]\n\t"
      "ds_read_b128 a[64:67], v155 offset:2048\n\t"
      "v_lshrrev_b32_e64 v143, 8, v111\n\t"
      "v_mfma_f32_16x16x32_bf16 a[128:131], a[4:7], v[16:19], a[128:131]\n\t"
      "buffer_load_dwordx4 v7, s[20:23], s50 offen offset:2048 lds\n\t"
      "ds_read_b128 a[68:71], v155 offset:4096\n\t"
      "v_mfma_f32_16x16x32_bf16 a[132:135], a[4:7], v[20:23], a[132:135]\n\t"
      "buffer_load_dwordx2 v[102:103], v14, s[24:27], s65 offen\n\t"
      "v_and_b32_e64 v136, v11, v110\n\t"
      "v_mfma_f32_16x16x32_bf16 a[136:139], a[8:11], v[16:19], a[136:139]\n\t"
      "ds_read_b128 a[72:75], v155 offset:6144\n\t"
      "v_and_b32_e64 v137, v12, v110\n\t"
      "v_mfma_f32_16x16x32_bf16 a[140:143], a[8:11], v[20:23], a[140:143]\n\t"
      "ds_read_b128 a[76:79], v155 offset:8192\n\t"
      "v_and_b32_e64 v140, v11, v111\n\t"
      "v_mfma_f32_16x16x32_bf16 v[160:163], a[12:15], v[16:19], v[160:163]\n\t"
      "ds_read_b128 a[80:83], v155 offset:10240\n\t"
      "v_and_b32_e64 v141, v12, v111\n\t"
      "v_mfma_f32_16x16x32_bf16 v[164:167], a[12:15], v[20:23], v[164:167]\n\t"
      "buffer_load_dwordx4 v8, s[20:23], s50 offen offset:3072 lds\n\t"
      "v_and_b32_e64 v138, v11, v139\n\t"
      "v_mfma_f32_16x16x32_bf16 v[168:171], a[16:19], v[16:19], v[168:171]\n\t"
      "ds_read_b128 a[84:87], v155 offset:12288\n\t"
      "v_and_b32_e64 v139, v12, v139\n\t"
      "v_mfma_f32_16x16x32_bf16 v[172:175], a[16:19], v[20:23], v[172:175]\n\t"
      "ds_read_b128 a[88:91], v155 offset:14336\n\t"
      "v_and_b32_e64 v142, v11, v143\n\t"
      "v_mfma_f32_16x16x32_bf16 v[176:179], a[20:23], v[16:19], v[176:179]\n\t"
      "ds_read_b128 a[92:95], v155 offset:16384\n\t"
      "v_and_b32_e64 v143, v12, v143\n\t"
      "v_mfma_f32_16x16x32_bf16 v[180:183], a[20:23], v[20:23], v[180:183]\n\t"
      "s_add_u32 m0, s19, 32768\n\t"
      "v_lshlrev_b32_e64 v120, 16, v112\n\t"
      "v_mfma_f32_16x16x32_bf16 v[184:187], a[24:27], v[16:19], v[184:187]\n\t"
      "buffer_load_dwordx4 v1, s[20:23], s51 offen offset:0 lds\n\t"
      "ds_read_b128 a[96:99], v155 offset:18432\n\t"
      "v_mfma_f32_16x16x32_bf16 v[188:191], a[24:27], v[20:23], v[188:191]\n\t"
      "ds_read_b128 a[100:103], v155 offset:20480\n\t"
      "v_and_b32_e64 v121, v10, v112\n\t"
      "v_mfma_f32_16x16x32_bf16 v[192:195], a[28:31], v[16:19], v[192:195]\n\t"
      "ds_read_b128 a[104:107], v155 offset:22528\n\t"
      "v_lshlrev_b32 v122, 16, v113\n\t"
      "v_and_b32 v123, v10, v113\n\t"
      "v_mfma_f32_16x16x32_bf16 v[196:199], a[28:31], v[20:23], v[196:199]\n\t"
      "v_fma_mix_f32 v144, v136, v124, v128 op_sel_hi:[1,0,0]\n\t"
      "v_fma_mix_f32 v145, v137, v126, v128 op_sel_hi:[1,0,0]\n\t"
      "v_mfma_f32_16x16x32_bf16 v[200:203], a[32:35], v[16:19], v[200:203]\n\t"
      "ds_read_b128 a[108:111], v155 offset:24576\n\t"
      "v_fma_mix_f32 v146, v138, v124, v128 op_sel_hi:[1,0,0]\n\t"
      "v_mfma_f32_16x16x32_bf16 v[204:207], a[32:35], v[20:23], v[204:207]\n\t"
      "ds_read_b128 a[112:115], v155 offset:26624\n\t"
      "v_fma_mix_f32 v147, v139, v126, v128 op_sel_hi:[1,0,0]\n\t"
      "v_mfma_f32_16x16x32_bf16 v[208:211], a[36:39], v[16:19], v[208:211]\n\t"
      "buffer_load_dwordx4 v2, s[20:23], s51 offen offset:1024 lds\n\t"
      "ds_read_b128 a[116:119], v155 offset:28672\n\t"
      "v_mfma_f32_16x16x32_bf16 v[212:215], a[36:39], v[20:23], v[212:215]\n\t"
      "v_fma_mix_f32 v148, v136, v124, v128 op_sel:[1,0,0] op_sel_hi:[1,0,0]\n\t"
      "v_fma_mix_f32 v149, v137, v126, v128 op_sel:[1,0,0] op_sel_hi:[1,0,0]\n\t"
      "v_mfma_f32_16x16x32_bf16 v[216:219], a[40:43], v[16:19], v[216:219]\n\t"
      "v_cvt_pk_bf16_f32 v24, v144, v145\n\t"
      "v_cvt_pk_bf16_f32 v25, v146, v147\n\t"
      "v_mfma_f32_16x16x32_bf16 v[220:223], a[40:43], v[20:23], v[220:223]\n\t"
      "v_fma_mix_f32 v150, v138, v124, v128 op_sel:[1,0,0] op_sel_hi:[1,0,0]\n\t"
      "v_fma_mix_f32 v151, v139, v126, v128 op_sel:[1,0,0] op_sel_hi:[1,0,0]\n\t"
      "v_mfma_f32_16x16x32_bf16 v[224:227], a[44:47], v[16:19], v[224:227]\n\t"
      "v_cvt_pk_bf16_f32 v26, v148, v149\n\t"
      "v_cvt_pk_bf16_f32 v27, v150, v151\n\t"
      "v_mfma_f32_16x16x32_bf16 v[228:231], a[44:47], v[20:23], v[228:231]\n\t"
      "buffer_load_dwordx4 v3, s[20:23], s51 offen offset:2048 lds\n\t"
      "v_fma_mix_f32 v144, v140, v125, v129 op_sel_hi:[1,0,0]\n\t"
      "v_mfma_f32_16x16x32_bf16 v[232:235], a[48:51], v[16:19], v[232:235]\n\t"
      "v_fma_mix_f32 v145, v141, v127, v129 op_sel_hi:[1,0,0]\n\t"
      "v_fma_mix_f32 v146, v142, v125, v129 op_sel_hi:[1,0,0]\n\t"
      "v_mfma_f32_16x16x32_bf16 v[236:239], a[48:51], v[20:23], v[236:239]\n\t"
      "v_fma_mix_f32 v147, v143, v127, v129 op_sel_hi:[1,0,0]\n\t"
      "v_fma_mix_f32 v148, v140, v125, v129 op_sel:[1,0,0] op_sel_hi:[1,0,0]\n\t"
      "v_mfma_f32_16x16x32_bf16 v[240:243], a[52:55], v[16:19], v[240:243]\n\t"
      "v_fma_mix_f32 v149, v141, v127, v129 op_sel:[1,0,0] op_sel_hi:[1,0,0]\n\t"
      "v_cvt_pk_bf16_f32 v28, v144, v145\n\t"
      "v_mfma_f32_16x16x32_bf16 v[244:247], a[52:55], v[20:23], v[244:247]\n\t"
      "v_cvt_pk_bf16_f32 v29, v146, v147\n\t"
      "v_fma_mix_f32 v150, v142, v125, v129 op_sel:[1,0,0] op_sel_hi:[1,0,0]\n\t"
      "v_mfma_f32_16x16x32_bf16 v[248:251], a[56:59], v[16:19], v[248:251]\n\t"
      "v_fma_mix_f32 v151, v143, v127, v129 op_sel:[1,0,0] op_sel_hi:[1,0,0]\n\t"
      "v_cvt_pk_bf16_f32 v30, v148, v149\n\t"
      "v_cvt_pk_bf16_f32 v31, v150, v151\n\t"
      "v_mfma_f32_16x16x32_bf16 v[252:255], a[56:59], v[20:23], v[252:255]\n\t"
      "s_waitcnt vmcnt(18) lgkmcnt(0)\n\t"
      "s_barrier\n\t"
      "v_mfma_f32_16x16x32_bf16 a[120:123], a[60:63], v[24:27], a[120:123]\n\t"
      "ds_read_b128 a[0:3], v154 offset:32768\n\t"
      "v_lshrrev_b32_e64 v139, 8, v96\n\t"
      "v_mfma_f32_16x16x32_bf16 a[124:127], a[60:63], v[28:31], a[124:127]\n\t"
      "ds_read_b128 a[4:7], v154 offset:34816\n\t"
      "v_lshrrev_b32_e64 v143, 8, v97\n\t"
      "v_mfma_f32_16x16x32_bf16 a[128:131], a[64:67], v[24:27], a[128:131]\n\t"
      "buffer_load_dwordx4 v4, s[20:23], s51 offen offset:3072 lds\n\t"
      "ds_read_b128 a[8:11], v154 offset:36864\n\t"
      "v_mfma_f32_16x16x32_bf16 a[132:135], a[64:67], v[28:31], a[132:135]\n\t"
      "buffer_load_dwordx2 v[104:105], v13, s[24:27], s66 offen\n\t"
      "v_and_b32_e64 v136, v11, v96\n\t"
      "v_mfma_f32_16x16x32_bf16 a[136:139], a[68:71], v[24:27], a[136:139]\n\t"
      "ds_read_b128 a[12:15], v154 offset:38912\n\t"
      "v_and_b32_e64 v137, v12, v96\n\t"
      "v_mfma_f32_16x16x32_bf16 a[140:143], a[68:71], v[28:31], a[140:143]\n\t"
      "ds_read_b128 a[16:19], v154 offset:40960\n\t"
      "v_and_b32 v140, v11, v97\n\t"
      "v_and_b32 v141, v12, v97\n\t"
      "v_mfma_f32_16x16x32_bf16 v[160:163], a[72:75], v[24:27], v[160:163]\n\t"
      "s_add_u32 m0, s19, 36864\n\t"
      "ds_read_b128 a[20:23], v154 offset:43008\n\t"
      "v_mfma_f32_16x16x32_bf16 v[164:167], a[72:75], v[28:31], v[164:167]\n\t"
      "buffer_load_dwordx4 v5, s[20:23], s51 offen offset:0 lds\n\t"
      "v_and_b32_e64 v138, v11, v139\n\t"
      "v_mfma_f32_16x16x32_bf16 v[168:171], a[76:79], v[24:27], v[168:171]\n\t"
      "ds_read_b128 a[24:27], v154 offset:45056\n\t"
      "v_and_b32_e64 v139, v12, v139\n\t"
      "v_mfma_f32_16x16x32_bf16 v[172:175], a[76:79], v[28:31], v[172:175]\n\t"
      "ds_read_b128 a[28:31], v154 offset:47104\n\t"
      "v_and_b32_e64 v142, v11, v143\n\t"
      "v_mfma_f32_16x16x32_bf16 v[176:179], a[80:83], v[24:27], v[176:179]\n\t"
      "ds_read_b128 a[32:35], v154 offset:49152\n\t"
      "v_and_b32_e64 v143, v12, v143\n\t"
      "v_mfma_f32_16x16x32_bf16 v[180:183], a[80:83], v[28:31], v[180:183]\n\t"
      "buffer_load_dword v118, v15, s[28:31], s83 offen\n\t"
      "v_mul_f32 v124, 0x4b800000, v120\n\t"
      "v_mul_f32 v126, 0x49800000, v120\n\t"
      "v_mfma_f32_16x16x32_bf16 v[184:187], a[84:87], v[24:27], v[184:187]\n\t"
      "buffer_load_dwordx4 v6, s[20:23], s51 offen offset:1024 lds\n\t"
      "ds_read_b128 a[36:39], v154 offset:51200\n\t"
      "v_mfma_f32_16x16x32_bf16 v[188:191], a[84:87], v[28:31], v[188:191]\n\t"
      "ds_read_b128 a[40:43], v154 offset:53248\n\t"
      "v_mul_f32_e64 v128, -v122, v120\n\t"
      "v_mfma_f32_16x16x32_bf16 v[192:195], a[88:91], v[24:27], v[192:195]\n\t"
      "ds_read_b128 a[44:47], v154 offset:55296\n\t"
      "v_mul_f32 v125, 0x4b800000, v121\n\t"
      "v_mfma_f32_16x16x32_bf16 v[196:199], a[88:91], v[28:31], v[196:199]\n\t"
      "v_mul_f32 v127, 0x49800000, v121\n\t"
      "v_mul_f32_e64 v129, -v123, v121\n\t"
      "v_mfma_f32_16x16x32_bf16 v[200:203], a[92:95], v[24:27], v[200:203]\n\t"
      "ds_read_b128 a[48:51], v154 offset:57344\n\t"
      "v_fma_mix_f32 v144, v136, v124, v128 op_sel_hi:[1,0,0]\n\t"
      "v_mfma_f32_16x16x32_bf16 v[204:207], a[92:95], v[28:31], v[204:207]\n\t"
      "ds_read_b128 a[52:55], v154 offset:59392\n\t"
      "v_fma_mix_f32 v145, v137, v126, v128 op_sel_hi:[1,0,0]\n\t"
      "v_fma_mix_f32 v146, v138, v124, v128 op_sel_hi:[1,0,0]\n\t"
      "v_mfma_f32_16x16x32_bf16 v[208:211], a[96:99], v[24:27], v[208:211]\n\t"
      "buffer_load_dwordx4 v7, s[20:23], s51 offen offset:2048 lds\n\t"
      "ds_read_b128 a[56:59], v154 offset:61440\n\t"
      "v_mfma_f32_16x16x32_bf16 v[212:215], a[96:99], v[28:31], v[212:215]\n\t"
      "buffer_load_dword v119, v15, s[4:7], s83 offen\n\t"
      "v_fma_mix_f32 v147, v139, v126, v128 op_sel_hi:[1,0,0]\n\t"
      "v_mfma_f32_16x16x32_bf16 v[216:219], a[100:103], v[24:27], v[216:219]\n\t"
      "v_fma_mix_f32 v148, v136, v124, v128 op_sel:[1,0,0] op_sel_hi:[1,0,0]\n\t"
      "v_fma_mix_f32 v149, v137, v126, v128 op_sel:[1,0,0] op_sel_hi:[1,0,0]\n\t"
      "v_mfma_f32_16x16x32_bf16 v[220:223], a[100:103], v[28:31], v[220:223]\n\t"
      "v_cvt_pk_bf16_f32 v16, v144, v145\n\t"
      "v_cvt_pk_bf16_f32 v17, v146, v147\n\t"
      "v_mfma_f32_16x16x32_bf16 v[224:227], a[104:107], v[24:27], v[224:227]\n\t"
      "v_fma_mix_f32 v150, v138, v124, v128 op_sel:[1,0,0] op_sel_hi:[1,0,0]\n\t"
      "v_fma_mix_f32 v151, v139, v126, v128 op_sel:[1,0,0] op_sel_hi:[1,0,0]\n\t"
      "v_mfma_f32_16x16x32_bf16 v[228:231], a[104:107], v[28:31], v[228:231]\n\t"
      "buffer_load_dwordx4 v8, s[20:23], s51 offen offset:3072 lds\n\t"
      "v_cvt_pk_bf16_f32 v18, v148, v149\n\t"
      "v_cvt_pk_bf16_f32 v19, v150, v151\n\t"
      "v_mfma_f32_16x16x32_bf16 v[232:235], a[108:111], v[24:27], v[232:235]\n\t"
      "v_fma_mix_f32 v144, v140, v125, v129 op_sel_hi:[1,0,0]\n\t"
      "v_fma_mix_f32 v145, v141, v127, v129 op_sel_hi:[1,0,0]\n\t"
      "v_mfma_f32_16x16x32_bf16 v[236:239], a[108:111], v[28:31], v[236:239]\n\t"
      "v_fma_mix_f32 v146, v142, v125, v129 op_sel_hi:[1,0,0]\n\t"
      "v_fma_mix_f32 v147, v143, v127, v129 op_sel_hi:[1,0,0]\n\t"
      "v_mfma_f32_16x16x32_bf16 v[240:243], a[112:115], v[24:27], v[240:243]\n\t"
      "v_fma_mix_f32 v148, v140, v125, v129 op_sel:[1,0,0] op_sel_hi:[1,0,0]\n\t"
      "v_fma_mix_f32 v149, v141, v127, v129 op_sel:[1,0,0] op_sel_hi:[1,0,0]\n\t"
      "v_mfma_f32_16x16x32_bf16 v[244:247], a[112:115], v[28:31], v[244:247]\n\t"
      "v_cvt_pk_bf16_f32 v20, v144, v145\n\t"
      "v_cvt_pk_bf16_f32 v21, v146, v147\n\t"
      "v_mfma_f32_16x16x32_bf16 v[248:251], a[116:119], v[24:27], v[248:251]\n\t"
      "v_fma_mix_f32 v150, v142, v125, v129 op_sel:[1,0,0] op_sel_hi:[1,0,0]\n\t"
      "v_fma_mix_f32 v151, v143, v127, v129 op_sel:[1,0,0] op_sel_hi:[1,0,0]\n\t"
      "v_cvt_pk_bf16_f32 v22, v148, v149\n\t"
      "v_mfma_f32_16x16x32_bf16 v[252:255], a[116:119], v[28:31], v[252:255]\n\t"
      "v_cvt_pk_bf16_f32 v23, v150, v151\n\t"
      "s_waitcnt lgkmcnt(0)\n\t"
      "s_nop 0\n\t"
      "v_mfma_f32_16x16x32_bf16 a[120:123], a[0:3], v[16:19], a[120:123]\n\t"
      "ds_read_b128 a[60:63], v155 offset:32768\n\t"
      "v_lshrrev_b32_e64 v139, 8, v98\n\t"
      "v_mfma_f32_16x16x32_bf16 a[124:127], a[0:3], v[20:23], a[124:127]\n\t"
      "s_add_u32 m0, s19, 65536\n\t"
      "ds_read_b128 a[64:67], v155 offset:34816\n\t"
      "v_mfma_f32_16x16x32_bf16 a[128:131], a[4:7], v[16:19], a[128:131]\n\t"
      "buffer_load_dwordx4 v1, s[20:23], s52 offen offset:0 lds\n\t"
      "ds_read_b128 a[68:71], v155 offset:36864\n\t"
      "v_mfma_f32_16x16x32_bf16 a[132:135], a[4:7], v[20:23], a[132:135]\n\t"
      "buffer_load_dwordx2 v[106:107], v14, s[24:27], s66 offen\n\t"
      "v_lshrrev_b32_e64 v143, 8, v99\n\t"
      "v_mfma_f32_16x16x32_bf16 a[136:139], a[8:11], v[16:19], a[136:139]\n\t"
      "ds_read_b128 a[72:75], v155 offset:38912\n\t"
      "v_and_b32_e64 v136, v11, v98\n\t"
      "v_mfma_f32_16x16x32_bf16 a[140:143], a[8:11], v[20:23], a[140:143]\n\t"
      "ds_read_b128 a[76:79], v155 offset:40960\n\t"
      "v_and_b32_e64 v137, v12, v98\n\t"
      "v_mfma_f32_16x16x32_bf16 v[160:163], a[12:15], v[16:19], v[160:163]\n\t"
      "ds_read_b128 a[80:83], v155 offset:43008\n\t"
      "v_and_b32_e64 v140, v11, v99\n\t"
      "v_mfma_f32_16x16x32_bf16 v[164:167], a[12:15], v[20:23], v[164:167]\n\t"
      "buffer_load_dwordx4 v2, s[20:23], s52 offen offset:1024 lds\n\t"
      "v_and_b32_e64 v141, v12, v99\n\t"
      "v_mfma_f32_16x16x32_bf16 v[168:171], a[16:19], v[16:19], v[168:171]\n\t"
      "ds_read_b128 a[84:87], v155 offset:45056\n\t"
      "v_and_b32_e64 v138, v11, v139\n\t"
      "v_mfma_f32_16x16x32_bf16 v[172:175], a[16:19], v[20:23], v[172:175]\n\t"
      "ds_read_b128 a[88:91], v155 offset:47104\n\t"
      "v_and_b32 v139, v12, v139\n\t"
      "v_and_b32 v142, v11, v143\n\t"
      "v_mfma_f32_16x16x32_bf16 v[176:179], a[20:23], v[16:19], v[176:179]\n\t"
      "ds_read_b128 a[92:95], v155 offset:49152\n\t"
      "v_and_b32_e64 v143, v12, v143\n\t"
      "v_mfma_f32_16x16x32_bf16 v[180:183], a[20:23], v[20:23], v[180:183]\n\t"
      "v_lshlrev_b32 v120, 16, v114\n\t"
      "v_and_b32 v121, v10, v114\n\t"
      "v_mfma_f32_16x16x32_bf16 v[184:187], a[24:27], v[16:19], v[184:187]\n\t"
      "buffer_load_dwordx4 v3, s[20:23], s52 offen offset:2048 lds\n\t"
      "ds_read_b128 a[96:99], v155 offset:51200\n\t"
      "v_mfma_f32_16x16x32_bf16 v[188:191], a[24:27], v[20:23], v[188:191]\n\t"
      "ds_read_b128 a[100:103], v155 offset:53248\n\t"
      "v_lshlrev_b32_e64 v122, 16, v115\n\t"
      "v_mfma_f32_16x16x32_bf16 v[192:195], a[28:31], v[16:19], v[192:195]\n\t"
      "ds_read_b128 a[104:107], v155 offset:55296\n\t"
      "v_and_b32_e64 v123, v10, v115\n\t"
      "v_mfma_f32_16x16x32_bf16 v[196:199], a[28:31], v[20:23], v[196:199]\n\t"
      "v_fma_mix_f32 v144, v136, v124, v128 op_sel_hi:[1,0,0]\n\t"
      "v_fma_mix_f32 v145, v137, v126, v128 op_sel_hi:[1,0,0]\n\t"
      "v_mfma_f32_16x16x32_bf16 v[200:203], a[32:35], v[16:19], v[200:203]\n\t"
      "ds_read_b128 a[108:111], v155 offset:57344\n\t"
      "v_fma_mix_f32 v146, v138, v124, v128 op_sel_hi:[1,0,0]\n\t"
      "v_mfma_f32_16x16x32_bf16 v[204:207], a[32:35], v[20:23], v[204:207]\n\t"
      "ds_read_b128 a[112:115], v155 offset:59392\n\t"
      "v_fma_mix_f32 v147, v139, v126, v128 op_sel_hi:[1,0,0]\n\t"
      "v_mfma_f32_16x16x32_bf16 v[208:211], a[36:39], v[16:19], v[208:211]\n\t"
      "buffer_load_dwordx4 v4, s[20:23], s52 offen offset:3072 lds\n\t"
      "ds_read_b128 a[116:119], v155 offset:61440\n\t"
      "v_mfma_f32_16x16x32_bf16 v[212:215], a[36:39], v[20:23], v[212:215]\n\t"
      "v_fma_mix_f32 v148, v136, v124, v128 op_sel:[1,0,0] op_sel_hi:[1,0,0]\n\t"
      "v_fma_mix_f32 v149, v137, v126, v128 op_sel:[1,0,0] op_sel_hi:[1,0,0]\n\t"
      "v_cvt_pk_bf16_f32 v24, v144, v145\n\t"
      "v_mfma_f32_16x16x32_bf16 v[216:219], a[40:43], v[16:19], v[216:219]\n\t"
      "v_cvt_pk_bf16_f32 v25, v146, v147\n\t"
      "v_fma_mix_f32 v150, v138, v124, v128 op_sel:[1,0,0] op_sel_hi:[1,0,0]\n\t"
      "v_mfma_f32_16x16x32_bf16 v[220:223], a[40:43], v[20:23], v[220:223]\n\t"
      "v_fma_mix_f32 v151, v139, v126, v128 op_sel:[1,0,0] op_sel_hi:[1,0,0]\n\t"
      "v_cvt_pk_bf16_f32 v26, v148, v149\n\t"
      "v_mfma_f32_16x16x32_bf16 v[224:227], a[44:47], v[16:19], v[224:227]\n\t"
      "s_add_u32 m0, s19, 69632\n\t"
      "v_cvt_pk_bf16_f32 v27, v150, v151\n\t"
      "v_mfma_f32_16x16x32_bf16 v[228:231], a[44:47], v[20:23], v[228:231]\n\t"
      "buffer_load_dwordx4 v5, s[20:23], s52 offen offset:0 lds\n\t"
      "v_fma_mix_f32 v144, v140, v125, v129 op_sel_hi:[1,0,0]\n\t"
      "v_mfma_f32_16x16x32_bf16 v[232:235], a[48:51], v[16:19], v[232:235]\n\t"
      "v_fma_mix_f32 v145, v141, v127, v129 op_sel_hi:[1,0,0]\n\t"
      "v_fma_mix_f32 v146, v142, v125, v129 op_sel_hi:[1,0,0]\n\t"
      "v_mfma_f32_16x16x32_bf16 v[236:239], a[48:51], v[20:23], v[236:239]\n\t"
      "v_fma_mix_f32 v147, v143, v127, v129 op_sel_hi:[1,0,0]\n\t"
      "v_fma_mix_f32 v148, v140, v125, v129 op_sel:[1,0,0] op_sel_hi:[1,0,0]\n\t"
      "v_mfma_f32_16x16x32_bf16 v[240:243], a[52:55], v[16:19], v[240:243]\n\t"
      "v_fma_mix_f32 v149, v141, v127, v129 op_sel:[1,0,0] op_sel_hi:[1,0,0]\n\t"
      "v_cvt_pk_bf16_f32 v28, v144, v145\n\t"
      "v_mfma_f32_16x16x32_bf16 v[244:247], a[52:55], v[20:23], v[244:247]\n\t"
      "v_cvt_pk_bf16_f32 v29, v146, v147\n\t"
      "v_fma_mix_f32 v150, v142, v125, v129 op_sel:[1,0,0] op_sel_hi:[1,0,0]\n\t"
      "v_mfma_f32_16x16x32_bf16 v[248:251], a[56:59], v[16:19], v[248:251]\n\t"
      "v_fma_mix_f32 v151, v143, v127, v129 op_sel:[1,0,0] op_sel_hi:[1,0,0]\n\t"
      "v_cvt_pk_bf16_f32 v30, v148, v149\n\t"
      "v_cvt_pk_bf16_f32 v31, v150, v151\n\t"
      "v_mfma_f32_16x16x32_bf16 v[252:255], a[56:59], v[20:23], v[252:255]\n\t"
      "s_waitcnt vmcnt(18) lgkmcnt(0)\n\t"
      "s_barrier\n\t"
      "v_mfma_f32_16x16x32_bf16 a[120:123], a[60:63], v[24:27], a[120:123]\n\t"
      "ds_read_b128 a[0:3], v156 offset:0\n\t"
      "v_lshrrev_b32_e64 v139, 8, v100\n\t"
      "v_mfma_f32_16x16x32_bf16 a[124:127], a[60:63], v[28:31], a[124:127]\n\t"
      "ds_read_b128 a[4:7], v156 offset:2048\n\t"
      "v_lshrrev_b32_e64 v143, 8, v101\n\t"
      "v_mfma_f32_16x16x32_bf16 a[128:131], a[64:67], v[24:27], a[128:131]\n\t"
      "buffer_load_dwordx4 v6, s[20:23], s52 offen offset:1024 lds\n\t"
      "ds_read_b128 a[8:11], v156 offset:4096\n\t"
      "v_mfma_f32_16x16x32_bf16 a[132:135], a[64:67], v[28:31], a[132:135]\n\t"
      "buffer_load_dwordx2 v[108:109], v13, s[24:27], s67 offen\n\t"
      "v_and_b32_e64 v136, v11, v100\n\t"
      "v_mfma_f32_16x16x32_bf16 a[136:139], a[68:71], v[24:27], a[136:139]\n\t"
      "ds_read_b128 a[12:15], v156 offset:6144\n\t"
      "v_and_b32_e64 v137, v12, v100\n\t"
      "v_mfma_f32_16x16x32_bf16 a[140:143], a[68:71], v[28:31], a[140:143]\n\t"
      "ds_read_b128 a[16:19], v156 offset:8192\n\t"
      "v_and_b32_e64 v140, v11, v101\n\t"
      "v_mfma_f32_16x16x32_bf16 v[160:163], a[72:75], v[24:27], v[160:163]\n\t"
      "ds_read_b128 a[20:23], v156 offset:10240\n\t"
      "v_and_b32_e64 v141, v12, v101\n\t"
      "v_mfma_f32_16x16x32_bf16 v[164:167], a[72:75], v[28:31], v[164:167]\n\t"
      "buffer_load_dwordx4 v7, s[20:23], s52 offen offset:2048 lds\n\t"
      "v_and_b32_e64 v138, v11, v139\n\t"
      "v_mfma_f32_16x16x32_bf16 v[168:171], a[76:79], v[24:27], v[168:171]\n\t"
      "ds_read_b128 a[24:27], v156 offset:12288\n\t"
      "v_and_b32_e64 v139, v12, v139\n\t"
      "v_mfma_f32_16x16x32_bf16 v[172:175], a[76:79], v[28:31], v[172:175]\n\t"
      "ds_read_b128 a[28:31], v156 offset:14336\n\t"
      "v_and_b32 v142, v11, v143\n\t"
      "v_and_b32 v143, v12, v143\n\t"
      "v_mfma_f32_16x16x32_bf16 v[176:179], a[80:83], v[24:27], v[176:179]\n\t"
      "ds_read_b128 a[32:35], v156 offset:16384\n\t"
      "v_mul_f32 v124, 0x4b800000, v120\n\t"
      "v_mfma_f32_16x16x32_bf16 v[180:183], a[80:83], v[28:31], v[180:183]\n\t"
      "v_mul_f32 v126, 0x49800000, v120\n\t"
      "v_mul_f32_e64 v128, -v122, v120\n\t"
      "v_mfma_f32_16x16x32_bf16 v[184:187], a[84:87], v[24:27], v[184:187]\n\t"
      "buffer_load_dwordx4 v8, s[20:23], s52 offen offset:3072 lds\n\t"
      "ds_read_b128 a[36:39], v156 offset:18432\n\t"
      "v_mfma_f32_16x16x32_bf16 v[188:191], a[84:87], v[28:31], v[188:191]\n\t"
      "ds_read_b128 a[40:43], v156 offset:20480\n\t"
      "v_mul_f32 v125, 0x4b800000, v121\n\t"
      "v_mfma_f32_16x16x32_bf16 v[192:195], a[88:91], v[24:27], v[192:195]\n\t"
      "ds_read_b128 a[44:47], v156 offset:22528\n\t"
      "v_mul_f32 v127, 0x49800000, v121\n\t"
      "v_mfma_f32_16x16x32_bf16 v[196:199], a[88:91], v[28:31], v[196:199]\n\t"
      "v_mul_f32_e64 v129, -v123, v121\n\t"
      "v_fma_mix_f32 v144, v136, v124, v128 op_sel_hi:[1,0,0]\n\t"
      "v_mfma_f32_16x16x32_bf16 v[200:203], a[92:95], v[24:27], v[200:203]\n\t"
      "ds_read_b128 a[48:51], v156 offset:24576\n\t"
      "v_fma_mix_f32 v145, v137, v126, v128 op_sel_hi:[1,0,0]\n\t"
      "v_mfma_f32_16x16x32_bf16 v[204:207], a[92:95], v[28:31], v[204:207]\n\t"
      "s_add_u32 m0, s19, 98304\n\t"
      "ds_read_b128 a[52:55], v156 offset:26624\n\t"
      "v_mfma_f32_16x16x32_bf16 v[208:211], a[96:99], v[24:27], v[208:211]\n\t"
      "buffer_load_dwordx4 v1, s[20:23], s53 offen offset:0 lds\n\t"
      "ds_read_b128 a[56:59], v156 offset:28672\n\t"
      "v_mfma_f32_16x16x32_bf16 v[212:215], a[96:99], v[28:31], v[212:215]\n\t"
      "v_fma_mix_f32 v146, v138, v124, v128 op_sel_hi:[1,0,0]\n\t"
      "v_fma_mix_f32 v147, v139, v126, v128 op_sel_hi:[1,0,0]\n\t"
      "v_fma_mix_f32 v148, v136, v124, v128 op_sel:[1,0,0] op_sel_hi:[1,0,0]\n\t"
      "v_mfma_f32_16x16x32_bf16 v[216:219], a[100:103], v[24:27], v[216:219]\n\t"
      "v_fma_mix_f32 v149, v137, v126, v128 op_sel:[1,0,0] op_sel_hi:[1,0,0]\n\t"
      "v_cvt_pk_bf16_f32 v16, v144, v145\n\t"
      "v_mfma_f32_16x16x32_bf16 v[220:223], a[100:103], v[28:31], v[220:223]\n\t"
      "v_cvt_pk_bf16_f32 v17, v146, v147\n\t"
      "v_fma_mix_f32 v150, v138, v124, v128 op_sel:[1,0,0] op_sel_hi:[1,0,0]\n\t"
      "v_mfma_f32_16x16x32_bf16 v[224:227], a[104:107], v[24:27], v[224:227]\n\t"
      "v_fma_mix_f32 v151, v139, v126, v128 op_sel:[1,0,0] op_sel_hi:[1,0,0]\n\t"
      "v_cvt_pk_bf16_f32 v18, v148, v149\n\t"
      "v_mfma_f32_16x16x32_bf16 v[228:231], a[104:107], v[28:31], v[228:231]\n\t"
      "buffer_load_dwordx4 v2, s[20:23], s53 offen offset:1024 lds\n\t"
      "v_cvt_pk_bf16_f32 v19, v150, v151\n\t"
      "v_mfma_f32_16x16x32_bf16 v[232:235], a[108:111], v[24:27], v[232:235]\n\t"
      "v_fma_mix_f32 v144, v140, v125, v129 op_sel_hi:[1,0,0]\n\t"
      "v_fma_mix_f32 v145, v141, v127, v129 op_sel_hi:[1,0,0]\n\t"
      "v_mfma_f32_16x16x32_bf16 v[236:239], a[108:111], v[28:31], v[236:239]\n\t"
      "v_fma_mix_f32 v146, v142, v125, v129 op_sel_hi:[1,0,0]\n\t"
      "v_fma_mix_f32 v147, v143, v127, v129 op_sel_hi:[1,0,0]\n\t"
      "v_mfma_f32_16x16x32_bf16 v[240:243], a[112:115], v[24:27], v[240:243]\n\t"
      "v_fma_mix_f32 v148, v140, v125, v129 op_sel:[1,0,0] op_sel_hi:[1,0,0]\n\t"
      "v_fma_mix_f32 v149, v141, v127, v129 op_sel:[1,0,0] op_sel_hi:[1,0,0]\n\t"
      "v_mfma_f32_16x16x32_bf16 v[244:247], a[112:115], v[28:31], v[244:247]\n\t"
      "v_cvt_pk_bf16_f32 v20, v144, v145\n\t"
      "v_cvt_pk_bf16_f32 v21, v146, v147\n\t"
      "v_mfma_f32_16x16x32_bf16 v[248:251], a[116:119], v[24:27], v[248:251]\n\t"
      "v_fma_mix_f32 v150, v142, v125, v129 op_sel:[1,0,0] op_sel_hi:[1,0,0]\n\t"
      "v_fma_mix_f32 v151, v143, v127, v129 op_sel:[1,0,0] op_sel_hi:[1,0,0]\n\t"
      "v_cvt_pk_bf16_f32 v22, v148, v149\n\t"
      "v_mfma_f32_16x16x32_bf16 v[252:255], a[116:119], v[28:31], v[252:255]\n\t"
      "v_cvt_pk_bf16_f32 v23, v150, v151\n\t"
      "s_waitcnt lgkmcnt(0)\n\t"
      "s_nop 0\n\t"
      "v_mfma_f32_16x16x32_bf16 a[120:123], a[0:3], v[16:19], a[120:123]\n\t"
      "ds_read_b128 a[60:63], v157 offset:0\n\t"
      "v_lshrrev_b32_e64 v139, 8, v102\n\t"
      "v_mfma_f32_16x16x32_bf16 a[124:127], a[0:3], v[20:23], a[124:127]\n\t"
      "ds_read_b128 a[64:67], v157 offset:2048\n\t"
      "v_lshrrev_b32_e64 v143, 8, v103\n\t"
      "v_mfma_f32_16x16x32_bf16 a[128:131], a[4:7], v[16:19], a[128:131]\n\t"
      "buffer_load_dwordx4 v3, s[20:23], s53 offen offset:2048 lds\n\t"
      "ds_read_b128 a[68:71], v157 offset:4096\n\t"
      "v_mfma_f32_16x16x32_bf16 a[132:135], a[4:7], v[20:23], a[132:135]\n\t"
      "buffer_load_dwordx2 v[110:111], v14, s[24:27], s67 offen\n\t"
      "v_and_b32_e64 v136, v11, v102\n\t"
      "v_mfma_f32_16x16x32_bf16 a[136:139], a[8:11], v[16:19], a[136:139]\n\t"
      "ds_read_b128 a[72:75], v157 offset:6144\n\t"
      "v_and_b32_e64 v137, v12, v102\n\t"
      "v_mfma_f32_16x16x32_bf16 a[140:143], a[8:11], v[20:23], a[140:143]\n\t"
      "ds_read_b128 a[76:79], v157 offset:8192\n\t"
      "v_and_b32_e64 v140, v11, v103\n\t"
      "v_mfma_f32_16x16x32_bf16 v[160:163], a[12:15], v[16:19], v[160:163]\n\t"
      "ds_read_b128 a[80:83], v157 offset:10240\n\t"
      "v_and_b32_e64 v141, v12, v103\n\t"
      "v_mfma_f32_16x16x32_bf16 v[164:167], a[12:15], v[20:23], v[164:167]\n\t"
      "buffer_load_dwordx4 v4, s[20:23], s53 offen offset:3072 lds\n\t"
      "v_and_b32_e64 v138, v11, v139\n\t"
      "v_mfma_f32_16x16x32_bf16 v[168:171], a[16:19], v[16:19], v[168:171]\n\t"
      "ds_read_b128 a[84:87], v157 offset:12288\n\t"
      "v_and_b32_e64 v139, v12, v139\n\t"
      "v_mfma_f32_16x16x32_bf16 v[172:175], a[16:19], v[20:23], v[172:175]\n\t"
      "ds_read_b128 a[88:91], v157 offset:14336\n\t"
      "v_and_b32_e64 v142, v11, v143\n\t"
      "v_mfma_f32_16x16x32_bf16 v[176:179], a[20:23], v[16:19], v[176:179]\n\t"
      "ds_read_b128 a[92:95], v157 offset:16384\n\t"
      "v_and_b32_e64 v143, v12, v143\n\t"
      "v_mfma_f32_16x16x32_bf16 v[180:183], a[20:23], v[20:23], v[180:183]\n\t"
      "s_add_u32 m0, s19, 102400\n\t"
      "v_lshlrev_b32_e64 v120, 16, v116\n\t"
      "v_mfma_f32_16x16x32_bf16 v[184:187], a[24:27], v[16:19], v[184:187]\n\t"
      "buffer_load_dwordx4 v5, s[20:23], s53 offen offset:0 lds\n\t"
      "ds_read_b128 a[96:99], v157 offset:18432\n\t"
      "v_mfma_f32_16x16x32_bf16 v[188:191], a[24:27], v[20:23], v[188:191]\n\t"
      "ds_read_b128 a[100:103], v157 offset:20480\n\t"
      "v_and_b32_e64 v121, v10, v116\n\t"
      "v_mfma_f32_16x16x32_bf16 v[192:195], a[28:31], v[16:19], v[192:195]\n\t"
      "ds_read_b128 a[104:107], v157 offset:22528\n\t"
      "v_lshlrev_b32 v122, 16, v117\n\t"
      "v_and_b32 v123, v10, v117\n\t"
      "v_mfma_f32_16x16x32_bf16 v[196:199], a[28:31], v[20:23], v[196:199]\n\t"
      "v_fma_mix_f32 v144, v136, v124, v128 op_sel_hi:[1,0,0]\n\t"
      "v_fma_mix_f32 v145, v137, v126, v128 op_sel_hi:[1,0,0]\n\t"
      "v_mfma_f32_16x16x32_bf16 v[200:203], a[32:35], v[16:19], v[200:203]\n\t"
      "ds_read_b128 a[108:111], v157 offset:24576\n\t"
      "v_fma_mix_f32 v146, v138, v124, v128 op_sel_hi:[1,0,0]\n\t"
      "v_mfma_f32_16x16x32_bf16 v[204:207], a[32:35], v[20:23], v[204:207]\n\t"
      "ds_read_b128 a[112:115], v157 offset:26624\n\t"
      "v_fma_mix_f32 v147, v139, v126, v128 op_sel_hi:[1,0,0]\n\t"
      "v_mfma_f32_16x16x32_bf16 v[208:211], a[36:39], v[16:19], v[208:211]\n\t"
      "buffer_load_dwordx4 v6, s[20:23], s53 offen offset:1024 lds\n\t"
      "ds_read_b128 a[116:119], v157 offset:28672\n\t"
      "v_mfma_f32_16x16x32_bf16 v[212:215], a[36:39], v[20:23], v[212:215]\n\t"
      "v_fma_mix_f32 v148, v136, v124, v128 op_sel:[1,0,0] op_sel_hi:[1,0,0]\n\t"
      "v_fma_mix_f32 v149, v137, v126, v128 op_sel:[1,0,0] op_sel_hi:[1,0,0]\n\t"
      "v_mfma_f32_16x16x32_bf16 v[216:219], a[40:43], v[16:19], v[216:219]\n\t"
      "v_cvt_pk_bf16_f32 v24, v144, v145\n\t"
      "v_cvt_pk_bf16_f32 v25, v146, v147\n\t"
      "v_mfma_f32_16x16x32_bf16 v[220:223], a[40:43], v[20:23], v[220:223]\n\t"
      "v_fma_mix_f32 v150, v138, v124, v128 op_sel:[1,0,0] op_sel_hi:[1,0,0]\n\t"
      "v_fma_mix_f32 v151, v139, v126, v128 op_sel:[1,0,0] op_sel_hi:[1,0,0]\n\t"
      "v_mfma_f32_16x16x32_bf16 v[224:227], a[44:47], v[16:19], v[224:227]\n\t"
      "v_cvt_pk_bf16_f32 v26, v148, v149\n\t"
      "v_cvt_pk_bf16_f32 v27, v150, v151\n\t"
      "v_mfma_f32_16x16x32_bf16 v[228:231], a[44:47], v[20:23], v[228:231]\n\t"
      "buffer_load_dwordx4 v7, s[20:23], s53 offen offset:2048 lds\n\t"
      "v_fma_mix_f32 v144, v140, v125, v129 op_sel_hi:[1,0,0]\n\t"
      "v_mfma_f32_16x16x32_bf16 v[232:235], a[48:51], v[16:19], v[232:235]\n\t"
      "v_fma_mix_f32 v145, v141, v127, v129 op_sel_hi:[1,0,0]\n\t"
      "v_fma_mix_f32 v146, v142, v125, v129 op_sel_hi:[1,0,0]\n\t"
      "v_mfma_f32_16x16x32_bf16 v[236:239], a[48:51], v[20:23], v[236:239]\n\t"
      "v_fma_mix_f32 v147, v143, v127, v129 op_sel_hi:[1,0,0]\n\t"
      "v_fma_mix_f32 v148, v140, v125, v129 op_sel:[1,0,0] op_sel_hi:[1,0,0]\n\t"
      "v_mfma_f32_16x16x32_bf16 v[240:243], a[52:55], v[16:19], v[240:243]\n\t"
      "v_fma_mix_f32 v149, v141, v127, v129 op_sel:[1,0,0] op_sel_hi:[1,0,0]\n\t"
      "v_cvt_pk_bf16_f32 v28, v144, v145\n\t"
      "v_mfma_f32_16x16x32_bf16 v[244:247], a[52:55], v[20:23], v[244:247]\n\t"
      "v_cvt_pk_bf16_f32 v29, v146, v147\n\t"
      "v_fma_mix_f32 v150, v142, v125, v129 op_sel:[1,0,0] op_sel_hi:[1,0,0]\n\t"
      "v_mfma_f32_16x16x32_bf16 v[248:251], a[56:59], v[16:19], v[248:251]\n\t"
      "v_fma_mix_f32 v151, v143, v127, v129 op_sel:[1,0,0] op_sel_hi:[1,0,0]\n\t"
      "v_cvt_pk_bf16_f32 v30, v148, v149\n\t"
      "v_cvt_pk_bf16_f32 v31, v150, v151\n\t"
      "v_mfma_f32_16x16x32_bf16 v[252:255], a[56:59], v[20:23], v[252:255]\n\t"
      "s_waitcnt vmcnt(16) lgkmcnt(0)\n\t"
      "s_barrier\n\t"
      "v_mfma_f32_16x16x32_bf16 a[120:123], a[60:63], v[24:27], a[120:123]\n\t"
      "ds_read_b128 a[0:3], v152 offset:0\n\t"
      "v_lshrrev_b32_e64 v139, 8, v104\n\t"
      "v_mfma_f32_16x16x32_bf16 a[124:127], a[60:63], v[28:31], a[124:127]\n\t"
      "ds_read_b128 a[4:7], v152 offset:2048\n\t"
      "v_lshrrev_b32_e64 v143, 8, v105\n\t"
      "v_mfma_f32_16x16x32_bf16 a[128:131], a[64:67], v[24:27], a[128:131]\n\t"
      "buffer_load_dwordx4 v8, s[20:23], s53 offen offset:3072 lds\n\t"
      "ds_read_b128 a[8:11], v152 offset:4096\n\t"
      "v_mfma_f32_16x16x32_bf16 a[132:135], a[64:67], v[28:31], a[132:135]\n\t"
      "v_and_b32 v136, v11, v104\n\t"
      "v_and_b32 v137, v12, v104\n\t"
      "v_mfma_f32_16x16x32_bf16 a[136:139], a[68:71], v[24:27], a[136:139]\n\t"
      "ds_read_b128 a[12:15], v152 offset:6144\n\t"
      "v_and_b32_e64 v140, v11, v105\n\t"
      "v_mfma_f32_16x16x32_bf16 a[140:143], a[68:71], v[28:31], a[140:143]\n\t"
      "ds_read_b128 a[16:19], v152 offset:8192\n\t"
      "v_and_b32_e64 v141, v12, v105\n\t"
      "v_mfma_f32_16x16x32_bf16 v[160:163], a[72:75], v[24:27], v[160:163]\n\t"
      "s_add_u32 m0, s19, 131072\n\t"
      "ds_read_b128 a[20:23], v152 offset:10240\n\t"
      "v_mfma_f32_16x16x32_bf16 v[164:167], a[72:75], v[28:31], v[164:167]\n\t"
      "buffer_load_dwordx4 v1, s[20:23], s54 offen offset:0 lds\n\t"
      "v_and_b32_e64 v138, v11, v139\n\t"
      "v_mfma_f32_16x16x32_bf16 v[168:171], a[76:79], v[24:27], v[168:171]\n\t"
      "ds_read_b128 a[24:27], v152 offset:12288\n\t"
      "v_and_b32_e64 v139, v12, v139\n\t"
      "v_mfma_f32_16x16x32_bf16 v[172:175], a[76:79], v[28:31], v[172:175]\n\t"
      "ds_read_b128 a[28:31], v152 offset:14336\n\t"
      "v_and_b32_e64 v142, v11, v143\n\t"
      "v_mfma_f32_16x16x32_bf16 v[176:179], a[80:83], v[24:27], v[176:179]\n\t"
      "ds_read_b128 a[32:35], v152 offset:16384\n\t"
      "v_and_b32_e64 v143, v12, v143\n\t"
      "v_mfma_f32_16x16x32_bf16 v[180:183], a[80:83], v[28:31], v[180:183]\n\t"
      "v_mul_f32 v124, 0x4b800000, v120\n\t"
      "v_mul_f32 v126, 0x49800000, v120\n\t"
      "v_mfma_f32_16x16x32_bf16 v[184:187], a[84:87], v[24:27], v[184:187]\n\t"
      "buffer_load_dwordx4 v2, s[20:23], s54 offen offset:1024 lds\n\t"
      "ds_read_b128 a[36:39], v152 offset:18432\n\t"
      "v_mfma_f32_16x16x32_bf16 v[188:191], a[84:87], v[28:31], v[188:191]\n\t"
      "ds_read_b128 a[40:43], v152 offset:20480\n\t"
      "v_mul_f32_e64 v128, -v122, v120\n\t"
      "v_mfma_f32_16x16x32_bf16 v[192:195], a[88:91], v[24:27], v[192:195]\n\t"
      "ds_read_b128 a[44:47], v152 offset:22528\n\t"
      "v_mul_f32 v125, 0x4b800000, v121\n\t"
      "v_mul_f32 v127, 0x49800000, v121\n\t"
      "v_mfma_f32_16x16x32_bf16 v[196:199], a[88:91], v[28:31], v[196:199]\n\t"
      "v_mul_f32_e64 v129, -v123, v121\n\t"
      "v_fma_mix_f32 v144, v136, v124, v128 op_sel_hi:[1,0,0]\n\t"
      "v_mfma_f32_16x16x32_bf16 v[200:203], a[92:95], v[24:27], v[200:203]\n\t"
      "ds_read_b128 a[48:51], v152 offset:24576\n\t"
      "v_fma_mix_f32 v145, v137, v126, v128 op_sel_hi:[1,0,0]\n\t"
      "v_mfma_f32_16x16x32_bf16 v[204:207], a[92:95], v[28:31], v[204:207]\n\t"
      "ds_read_b128 a[52:55], v152 offset:26624\n\t"
      "v_fma_mix_f32 v146, v138, v124, v128 op_sel_hi:[1,0,0]\n\t"
      "v_mfma_f32_16x16x32_bf16 v[208:211], a[96:99], v[24:27], v[208:211]\n\t"
      "buffer_load_dwordx4 v3, s[20:23], s54 offen offset:2048 lds\n\t"
      "ds_read_b128 a[56:59], v152 offset:28672\n\t"
      "v_mfma_f32_16x16x32_bf16 v[212:215], a[96:99], v[28:31], v[212:215]\n\t"
      "v_fma_mix_f32 v147, v139, v126, v128 op_sel_hi:[1,0,0]\n\t"
      "v_fma_mix_f32 v148, v136, v124, v128 op_sel:[1,0,0] op_sel_hi:[1,0,0]\n\t"
      "v_mfma_f32_16x16x32_bf16 v[216:219], a[100:103], v[24:27], v[216:219]\n\t"
      "v_fma_mix_f32 v149, v137, v126, v128 op_sel:[1,0,0] op_sel_hi:[1,0,0]\n\t"
      "v_cvt_pk_bf16_f32 v16, v144, v145\n\t"
      "v_mfma_f32_16x16x32_bf16 v[220:223], a[100:103], v[28:31], v[220:223]\n\t"
      "v_cvt_pk_bf16_f32 v17, v146, v147\n\t"
      "v_fma_mix_f32 v150, v138, v124, v128 op_sel:[1,0,0] op_sel_hi:[1,0,0]\n\t"
      "v_mfma_f32_16x16x32_bf16 v[224:227], a[104:107], v[24:27], v[224:227]\n\t"
      "v_fma_mix_f32 v151, v139, v126, v128 op_sel:[1,0,0] op_sel_hi:[1,0,0]\n\t"
      "v_cvt_pk_bf16_f32 v18, v148, v149\n\t"
      "v_mfma_f32_16x16x32_bf16 v[228:231], a[104:107], v[28:31], v[228:231]\n\t"
      "buffer_load_dwordx4 v4, s[20:23], s54 offen offset:3072 lds\n\t"
      "v_cvt_pk_bf16_f32 v19, v150, v151\n\t"
      "v_mfma_f32_16x16x32_bf16 v[232:235], a[108:111], v[24:27], v[232:235]\n\t"
      "v_fma_mix_f32 v144, v140, v125, v129 op_sel_hi:[1,0,0]\n\t"
      "v_fma_mix_f32 v145, v141, v127, v129 op_sel_hi:[1,0,0]\n\t"
      "v_mfma_f32_16x16x32_bf16 v[236:239], a[108:111], v[28:31], v[236:239]\n\t"
      "v_fma_mix_f32 v146, v142, v125, v129 op_sel_hi:[1,0,0]\n\t"
      "v_fma_mix_f32 v147, v143, v127, v129 op_sel_hi:[1,0,0]\n\t"
      "v_mfma_f32_16x16x32_bf16 v[240:243], a[112:115], v[24:27], v[240:243]\n\t"
      "v_fma_mix_f32 v148, v140, v125, v129 op_sel:[1,0,0] op_sel_hi:[1,0,0]\n\t"
      "v_fma_mix_f32 v149, v141, v127, v129 op_sel:[1,0,0] op_sel_hi:[1,0,0]\n\t"
      "v_mfma_f32_16x16x32_bf16 v[244:247], a[112:115], v[28:31], v[244:247]\n\t"
      "v_cvt_pk_bf16_f32 v20, v144, v145\n\t"
      "v_cvt_pk_bf16_f32 v21, v146, v147\n\t"
      "v_mfma_f32_16x16x32_bf16 v[248:251], a[116:119], v[24:27], v[248:251]\n\t"
      "v_fma_mix_f32 v150, v142, v125, v129 op_sel:[1,0,0] op_sel_hi:[1,0,0]\n\t"
      "v_fma_mix_f32 v151, v143, v127, v129 op_sel:[1,0,0] op_sel_hi:[1,0,0]\n\t"
      "v_cvt_pk_bf16_f32 v22, v148, v149\n\t"
      "v_mfma_f32_16x16x32_bf16 v[252:255], a[116:119], v[28:31], v[252:255]\n\t"
      "v_cvt_pk_bf16_f32 v23, v150, v151\n\t"
      "s_waitcnt lgkmcnt(0)\n\t"
      "s_nop 0\n\t"
      "v_mfma_f32_16x16x32_bf16 a[120:123], a[0:3], v[16:19], a[120:123]\n\t"
      "ds_read_b128 a[60:63], v153 offset:0\n\t"
      "v_lshrrev_b32_e64 v139, 8, v106\n\t"
      "v_mfma_f32_16x16x32_bf16 a[124:127], a[0:3], v[20:23], a[124:127]\n\t"
      "s_add_u32 m0, s19, 135168\n\t"
      "ds_read_b128 a[64:67], v153 offset:2048\n\t"
      "v_mfma_f32_16x16x32_bf16 a[128:131], a[4:7], v[16:19], a[128:131]\n\t"
      "buffer_load_dwordx4 v5, s[20:23], s54 offen offset:0 lds\n\t"
      "ds_read_b128 a[68:71], v153 offset:4096\n\t"
      "v_mfma_f32_16x16x32_bf16 a[132:135], a[4:7], v[20:23], a[132:135]\n\t"
      "v_lshrrev_b32 v143, 8, v107\n\t"
      "v_and_b32 v136, v11, v106\n\t"
      "v_mfma_f32_16x16x32_bf16 a[136:139], a[8:11], v[16:19], a[136:139]\n\t"
      "ds_read_b128 a[72:75], v153 offset:6144\n\t"
      "v_and_b32_e64 v137, v12, v106\n\t"
      "v_mfma_f32_16x16x32_bf16 a[140:143], a[8:11], v[20:23], a[140:143]\n\t"
      "ds_read_b128 a[76:79], v153 offset:8192\n\t"
      "v_and_b32_e64 v140, v11, v107\n\t"
      "v_mfma_f32_16x16x32_bf16 v[160:163], a[12:15], v[16:19], v[160:163]\n\t"
      "ds_read_b128 a[80:83], v153 offset:10240\n\t"
      "v_and_b32_e64 v141, v12, v107\n\t"
      "v_mfma_f32_16x16x32_bf16 v[164:167], a[12:15], v[20:23], v[164:167]\n\t"
      "v_and_b32 v138, v11, v139\n\t"
      "v_and_b32 v139, v12, v139\n\t"
      "v_mfma_f32_16x16x32_bf16 v[168:171], a[16:19], v[16:19], v[168:171]\n\t"
      "buffer_load_dwordx4 v6, s[20:23], s54 offen offset:1024 lds\n\t"
      "ds_read_b128 a[84:87], v153 offset:12288\n\t"
      "v_mfma_f32_16x16x32_bf16 v[172:175], a[16:19], v[20:23], v[172:175]\n\t"
      "ds_read_b128 a[88:91], v153 offset:14336\n\t"
      "v_and_b32_e64 v142, v11, v143\n\t"
      "v_mfma_f32_16x16x32_bf16 v[176:179], a[20:23], v[16:19], v[176:179]\n\t"
      "ds_read_b128 a[92:95], v153 offset:16384\n\t"
      "v_and_b32_e64 v143, v12, v143\n\t"
      "v_mfma_f32_16x16x32_bf16 v[180:183], a[20:23], v[20:23], v[180:183]\n\t"
      "v_lshlrev_b32 v120, 16, v118\n\t"
      "v_and_b32 v121, v10, v118\n\t"
      "v_mfma_f32_16x16x32_bf16 v[184:187], a[24:27], v[16:19], v[184:187]\n\t"
      "ds_read_b128 a[96:99], v153 offset:18432\n\t"
      "v_lshlrev_b32_e64 v122, 16, v119\n\t"
      "v_mfma_f32_16x16x32_bf16 v[188:191], a[24:27], v[20:23], v[188:191]\n\t"
      "ds_read_b128 a[100:103], v153 offset:20480\n\t"
      "v_and_b32_e64 v123, v10, v119\n\t"
      "v_mfma_f32_16x16x32_bf16 v[192:195], a[28:31], v[16:19], v[192:195]\n\t"
      "ds_read_b128 a[104:107], v153 offset:22528\n\t"
      "v_fma_mix_f32 v144, v136, v124, v128 op_sel_hi:[1,0,0]\n\t"
      "v_mfma_f32_16x16x32_bf16 v[196:199], a[28:31], v[20:23], v[196:199]\n\t"
      "buffer_load_dwordx4 v7, s[20:23], s54 offen offset:2048 lds\n\t"
      "v_fma_mix_f32 v145, v137, v126, v128 op_sel_hi:[1,0,0]\n\t"
      "v_mfma_f32_16x16x32_bf16 v[200:203], a[32:35], v[16:19], v[200:203]\n\t"
      "ds_read_b128 a[108:111], v153 offset:24576\n\t"
      "v_fma_mix_f32 v146, v138, v124, v128 op_sel_hi:[1,0,0]\n\t"
      "v_mfma_f32_16x16x32_bf16 v[204:207], a[32:35], v[20:23], v[204:207]\n\t"
      "ds_read_b128 a[112:115], v153 offset:26624\n\t"
      "v_fma_mix_f32 v147, v139, v126, v128 op_sel_hi:[1,0,0]\n\t"
      "v_mfma_f32_16x16x32_bf16 v[208:211], a[36:39], v[16:19], v[208:211]\n\t"
      "ds_read_b128 a[116:119], v153 offset:28672\n\t"
      "v_fma_mix_f32 v148, v136, v124, v128 op_sel:[1,0,0] op_sel_hi:[1,0,0]\n\t"
      "v_mfma_f32_16x16x32_bf16 v[212:215], a[36:39], v[20:23], v[212:215]\n\t"
      "v_fma_mix_f32 v149, v137, v126, v128 op_sel:[1,0,0] op_sel_hi:[1,0,0]\n\t"
      "v_cvt_pk_bf16_f32 v24, v144, v145\n\t"
      "v_mfma_f32_16x16x32_bf16 v[216:219], a[40:43], v[16:19], v[216:219]\n\t"
      "v_cvt_pk_bf16_f32 v25, v146, v147\n\t"
      "v_fma_mix_f32 v150, v138, v124, v128 op_sel:[1,0,0] op_sel_hi:[1,0,0]\n\t"
      "v_mfma_f32_16x16x32_bf16 v[220:223], a[40:43], v[20:23], v[220:223]\n\t"
      "v_fma_mix_f32 v151, v139, v126, v128 op_sel:[1,0,0] op_sel_hi:[1,0,0]\n\t"
      "v_cvt_pk_bf16_f32 v26, v148, v149\n\t"
      "v_mfma_f32_16x16x32_bf16 v[224:227], a[44:47], v[16:19], v[224:227]\n\t"
      "buffer_load_dwordx4 v8, s[20:23], s54 offen offset:3072 lds\n\t"
      "v_cvt_pk_bf16_f32 v27, v150, v151\n\t"
      "v_mfma_f32_16x16x32_bf16 v[228:231], a[44:47], v[20:23], v[228:231]\n\t"
      "v_fma_mix_f32 v144, v140, v125, v129 op_sel_hi:[1,0,0]\n\t"
      "v_fma_mix_f32 v145, v141, v127, v129 op_sel_hi:[1,0,0]\n\t"
      "v_mfma_f32_16x16x32_bf16 v[232:235], a[48:51], v[16:19], v[232:235]\n\t"
      "v_fma_mix_f32 v146, v142, v125, v129 op_sel_hi:[1,0,0]\n\t"
      "v_fma_mix_f32 v147, v143, v127, v129 op_sel_hi:[1,0,0]\n\t"
      "v_mfma_f32_16x16x32_bf16 v[236:239], a[48:51], v[20:23], v[236:239]\n\t"
      "v_fma_mix_f32 v148, v140, v125, v129 op_sel:[1,0,0] op_sel_hi:[1,0,0]\n\t"
      "v_fma_mix_f32 v149, v141, v127, v129 op_sel:[1,0,0] op_sel_hi:[1,0,0]\n\t"
      "v_mfma_f32_16x16x32_bf16 v[240:243], a[52:55], v[16:19], v[240:243]\n\t"
      "v_cvt_pk_bf16_f32 v28, v144, v145\n\t"
      "v_cvt_pk_bf16_f32 v29, v146, v147\n\t"
      "v_mfma_f32_16x16x32_bf16 v[244:247], a[52:55], v[20:23], v[244:247]\n\t"
      "v_fma_mix_f32 v150, v142, v125, v129 op_sel:[1,0,0] op_sel_hi:[1,0,0]\n\t"
      "v_fma_mix_f32 v151, v143, v127, v129 op_sel:[1,0,0] op_sel_hi:[1,0,0]\n\t"
      "v_mfma_f32_16x16x32_bf16 v[248:251], a[56:59], v[16:19], v[248:251]\n\t"
      "v_cvt_pk_bf16_f32 v30, v148, v149\n\t"
      "v_cvt_pk_bf16_f32 v31, v150, v151\n\t"
      "v_mfma_f32_16x16x32_bf16 v[252:255], a[56:59], v[20:23], v[252:255]\n\t"
      "s_waitcnt vmcnt(13) lgkmcnt(0)\n\t"
      "s_barrier\n\t"
      "v_mfma_f32_16x16x32_bf16 a[120:123], a[60:63], v[24:27], a[120:123]\n\t"
      "ds_read_b128 a[0:3], v152 offset:32768\n\t"
      "v_lshrrev_b32_e64 v139, 8, v108\n\t"
      "v_mfma_f32_16x16x32_bf16 a[124:127], a[60:63], v[28:31], a[124:127]\n\t"
      "s_add_u32 m0, s19, 0\n\t"
      "s_nop 0\n\t"
      "ds_read_b128 a[4:7], v152 offset:34816\n\t"
      "v_mfma_f32_16x16x32_bf16 a[128:131], a[64:67], v[24:27], a[128:131]\n\t"
      "buffer_load_dwordx4 v1, s[20:23], s55 offen offset:0 lds\n\t"
      "ds_read_b128 a[8:11], v152 offset:36864\n\t"
      "v_mfma_f32_16x16x32_bf16 a[132:135], a[64:67], v[28:31], a[132:135]\n\t"
      "v_lshrrev_b32 v143, 8, v109\n\t"
      "v_and_b32 v136, v11, v108\n\t"
      "v_mfma_f32_16x16x32_bf16 a[136:139], a[68:71], v[24:27], a[136:139]\n\t"
      "ds_read_b128 a[12:15], v152 offset:38912\n\t"
      "v_and_b32 v137, v12, v108\n\t"
      "v_and_b32 v140, v11, v109\n\t"
      "v_mfma_f32_16x16x32_bf16 a[140:143], a[68:71], v[28:31], a[140:143]\n\t"
      "buffer_load_dwordx4 v2, s[20:23], s55 offen offset:1024 lds\n\t"
      "ds_read_b128 a[16:19], v152 offset:40960\n\t"
      "v_mfma_f32_16x16x32_bf16 v[160:163], a[72:75], v[24:27], v[160:163]\n\t"
      "ds_read_b128 a[20:23], v152 offset:43008\n\t"
      "v_and_b32_e64 v141, v12, v109\n\t"
      "v_mfma_f32_16x16x32_bf16 v[164:167], a[72:75], v[28:31], v[164:167]\n\t"
      "v_and_b32 v138, v11, v139\n\t"
      "v_and_b32 v139, v12, v139\n\t"
      "v_mfma_f32_16x16x32_bf16 v[168:171], a[76:79], v[24:27], v[168:171]\n\t"
      "buffer_load_dwordx4 v3, s[20:23], s55 offen offset:2048 lds\n\t"
      "ds_read_b128 a[24:27], v152 offset:45056\n\t"
      "v_mfma_f32_16x16x32_bf16 v[172:175], a[76:79], v[28:31], v[172:175]\n\t"
      "ds_read_b128 a[28:31], v152 offset:47104\n\t"
      "v_and_b32 v142, v11, v143\n\t"
      "v_and_b32 v143, v12, v143\n\t"
      "v_mfma_f32_16x16x32_bf16 v[176:179], a[80:83], v[24:27], v[176:179]\n\t"
      "ds_read_b128 a[32:35], v152 offset:49152\n\t"
      "v_mul_f32 v124, 0x4b800000, v120\n\t"
      "v_mfma_f32_16x16x32_bf16 v[180:183], a[80:83], v[28:31], v[180:183]\n\t"
      "v_mul_f32 v126, 0x49800000, v120\n\t"
      "v_mul_f32_e64 v128, -v122, v120\n\t"
      "v_mfma_f32_16x16x32_bf16 v[184:187], a[84:87], v[24:27], v[184:187]\n\t"
      "buffer_load_dwordx4 v4, s[20:23], s55 offen offset:3072 lds\n\t"
      "ds_read_b128 a[36:39], v152 offset:51200\n\t"
      "v_mfma_f32_16x16x32_bf16 v[188:191], a[84:87], v[28:31], v[188:191]\n\t"
      "ds_read_b128 a[40:43], v152 offset:53248\n\t"
      "v_mul_f32 v125, 0x4b800000, v121\n\t"
      "v_mfma_f32_16x16x32_bf16 v[192:195], a[88:91], v[24:27], v[192:195]\n\t"
      "s_add_u32 m0, s19, 4096\n\t"
      "ds_read_b128 a[44:47], v152 offset:55296\n\t"
      "v_mul_f32 v127, 0x49800000, v121\n\t"
      "v_mfma_f32_16x16x32_bf16 v[196:199], a[88:91], v[28:31], v[196:199]\n\t"
      "buffer_load_dwordx4 v5, s[20:23], s55 offen offset:0 lds\n\t"
      "v_mul_f32_e64 v129, -v123, v121\n\t"
      "v_mfma_f32_16x16x32_bf16 v[200:203], a[92:95], v[24:27], v[200:203]\n\t"
      "ds_read_b128 a[48:51], v152 offset:57344\n\t"
      "v_fma_mix_f32 v144, v136, v124, v128 op_sel_hi:[1,0,0]\n\t"
      "v_mfma_f32_16x16x32_bf16 v[204:207], a[92:95], v[28:31], v[204:207]\n\t"
      "ds_read_b128 a[52:55], v152 offset:59392\n\t"
      "v_fma_mix_f32 v145, v137, v126, v128 op_sel_hi:[1,0,0]\n\t"
      "v_mfma_f32_16x16x32_bf16 v[208:211], a[96:99], v[24:27], v[208:211]\n\t"
      "buffer_load_dwordx4 v6, s[20:23], s55 offen offset:1024 lds\n\t"
      "ds_read_b128 a[56:59], v152 offset:61440\n\t"
      "v_mfma_f32_16x16x32_bf16 v[212:215], a[96:99], v[28:31], v[212:215]\n\t"
      "v_fma_mix_f32 v146, v138, v124, v128 op_sel_hi:[1,0,0]\n\t"
      "v_fma_mix_f32 v147, v139, v126, v128 op_sel_hi:[1,0,0]\n\t"
      "v_fma_mix_f32 v148, v136, v124, v128 op_sel:[1,0,0] op_sel_hi:[1,0,0]\n\t"
      "v_mfma_f32_16x16x32_bf16 v[216:219], a[100:103], v[24:27], v[216:219]\n\t"
      "v_fma_mix_f32 v149, v137, v126, v128 op_sel:[1,0,0] op_sel_hi:[1,0,0]\n\t"
      "v_cvt_pk_bf16_f32 v16, v144, v145\n\t"
      "v_mfma_f32_16x16x32_bf16 v[220:223], a[100:103], v[28:31], v[220:223]\n\t"
      "v_cvt_pk_bf16_f32 v17, v146, v147\n\t"
      "v_fma_mix_f32 v150, v138, v124, v128 op_sel:[1,0,0] op_sel_hi:[1,0,0]\n\t"
      "v_mfma_f32_16x16x32_bf16 v[224:227], a[104:107], v[24:27], v[224:227]\n\t"
      "buffer_load_dwordx4 v7, s[20:23], s55 offen offset:2048 lds\n\t"
      "v_fma_mix_f32 v151, v139, v126, v128 op_sel:[1,0,0] op_sel_hi:[1,0,0]\n\t"
      "v_mfma_f32_16x16x32_bf16 v[228:231], a[104:107], v[28:31], v[228:231]\n\t"
      "v_cvt_pk_bf16_f32 v18, v148, v149\n\t"
      "v_cvt_pk_bf16_f32 v19, v150, v151\n\t"
      "v_mfma_f32_16x16x32_bf16 v[232:235], a[108:111], v[24:27], v[232:235]\n\t"
      "v_fma_mix_f32 v144, v140, v125, v129 op_sel_hi:[1,0,0]\n\t"
      "v_fma_mix_f32 v145, v141, v127, v129 op_sel_hi:[1,0,0]\n\t"
      "v_fma_mix_f32 v146, v142, v125, v129 op_sel_hi:[1,0,0]\n\t"
      "v_mfma_f32_16x16x32_bf16 v[236:239], a[108:111], v[28:31], v[236:239]\n\t"
      "buffer_load_dwordx4 v8, s[20:23], s55 offen offset:3072 lds\n\t"
      "v_fma_mix_f32 v147, v143, v127, v129 op_sel_hi:[1,0,0]\n\t"
      "v_mfma_f32_16x16x32_bf16 v[240:243], a[112:115], v[24:27], v[240:243]\n\t"
      "v_fma_mix_f32 v148, v140, v125, v129 op_sel:[1,0,0] op_sel_hi:[1,0,0]\n\t"
      "v_fma_mix_f32 v149, v141, v127, v129 op_sel:[1,0,0] op_sel_hi:[1,0,0]\n\t"
      "v_mfma_f32_16x16x32_bf16 v[244:247], a[112:115], v[28:31], v[244:247]\n\t"
      "v_cvt_pk_bf16_f32 v20, v144, v145\n\t"
      "v_cvt_pk_bf16_f32 v21, v146, v147\n\t"
      "v_mfma_f32_16x16x32_bf16 v[248:251], a[116:119], v[24:27], v[248:251]\n\t"
      "v_fma_mix_f32 v150, v142, v125, v129 op_sel:[1,0,0] op_sel_hi:[1,0,0]\n\t"
      "v_fma_mix_f32 v151, v143, v127, v129 op_sel:[1,0,0] op_sel_hi:[1,0,0]\n\t"
      "v_cvt_pk_bf16_f32 v22, v148, v149\n\t"
      "v_mfma_f32_16x16x32_bf16 v[252:255], a[116:119], v[28:31], v[252:255]\n\t"
      "v_cvt_pk_bf16_f32 v23, v150, v151\n\t"
      "s_waitcnt lgkmcnt(0)\n\t"
      "s_nop 0\n\t"
      "v_mfma_f32_16x16x32_bf16 a[120:123], a[0:3], v[16:19], a[120:123]\n\t"
      "v_lshrrev_b32 v139, 8, v110\n\t"
      "v_lshrrev_b32 v143, 8, v111\n\t"
      "v_mfma_f32_16x16x32_bf16 a[124:127], a[0:3], v[20:23], a[124:127]\n\t"
      "v_and_b32 v136, v11, v110\n\t"
      "v_and_b32 v137, v12, v110\n\t"
      "v_mfma_f32_16x16x32_bf16 a[128:131], a[4:7], v[16:19], a[128:131]\n\t"
      "v_and_b32 v140, v11, v111\n\t"
      "v_and_b32 v141, v12, v111\n\t"
      "v_mfma_f32_16x16x32_bf16 a[132:135], a[4:7], v[20:23], a[132:135]\n\t"
      "v_and_b32 v138, v11, v139\n\t"
      "v_and_b32 v139, v12, v139\n\t"
      "v_mfma_f32_16x16x32_bf16 a[136:139], a[8:11], v[16:19], a[136:139]\n\t"
      "v_and_b32 v142, v11, v143\n\t"
      "v_and_b32 v143, v12, v143\n\t"
      "v_mfma_f32_16x16x32_bf16 a[140:143], a[8:11], v[20:23], a[140:143]\n\t"
      "v_fma_mix_f32 v144, v136, v124, v128 op_sel_hi:[1,0,0]\n\t"
      "v_fma_mix_f32 v145, v137, v126, v128 op_sel_hi:[1,0,0]\n\t"
      "v_mfma_f32_16x16x32_bf16 v[160:163], a[12:15], v[16:19], v[160:163]\n\t"
      "v_fma_mix_f32 v146, v138, v124, v128 op_sel_hi:[1,0,0]\n\t"
      "v_fma_mix_f32 v147, v139, v126, v128 op_sel_hi:[1,0,0]\n\t"
      "v_mfma_f32_16x16x32_bf16 v[164:167], a[12:15], v[20:23], v[164:167]\n\t"
      "v_fma_mix_f32 v148, v136, v124, v128 op_sel:[1,0,0] op_sel_hi:[1,0,0]\n\t"
      "v_fma_mix_f32 v149, v137, v126, v128 op_sel:[1,0,0] op_sel_hi:[1,0,0]\n\t"
      "v_mfma_f32_16x16x32_bf16 v[168:171], a[16:19], v[16:19], v[168:171]\n\t"
      "v_cvt_pk_bf16_f32 v24, v144, v145\n\t"
      "v_cvt_pk_bf16_f32 v25, v146, v147\n\t"
      "v_mfma_f32_16x16x32_bf16 v[172:175], a[16:19], v[20:23], v[172:175]\n\t"
      "v_fma_mix_f32 v150, v138, v124, v128 op_sel:[1,0,0] op_sel_hi:[1,0,0]\n\t"
      "v_fma_mix_f32 v151, v139, v126, v128 op_sel:[1,0,0] op_sel_hi:[1,0,0]\n\t"
      "v_mfma_f32_16x16x32_bf16 v[176:179], a[20:23], v[16:19], v[176:179]\n\t"
      "v_cvt_pk_bf16_f32 v26, v148, v149\n\t"
      "v_cvt_pk_bf16_f32 v27, v150, v151\n\t"
      "v_mfma_f32_16x16x32_bf16 v[180:183], a[20:23], v[20:23], v[180:183]\n\t"
      "v_fma_mix_f32 v144, v140, v125, v129 op_sel_hi:[1,0,0]\n\t"
      "v_fma_mix_f32 v145, v141, v127, v129 op_sel_hi:[1,0,0]\n\t"
      "v_mfma_f32_16x16x32_bf16 v[184:187], a[24:27], v[16:19], v[184:187]\n\t"
      "s_waitcnt vmcnt(4)\n\t"
      "s_barrier\n\t"
      "v_mfma_f32_16x16x32_bf16 v[188:191], a[24:27], v[20:23], v[188:191]\n\t"
      "ds_read_b128 a[60:63], v153 offset:32768\n\t"
      "v_fma_mix_f32 v146, v142, v125, v129 op_sel_hi:[1,0,0]\n\t"
      "v_mfma_f32_16x16x32_bf16 v[192:195], a[28:31], v[16:19], v[192:195]\n\t"
      "ds_read_b128 a[64:67], v154 offset:0\n\t"
      "v_fma_mix_f32 v147, v143, v127, v129 op_sel_hi:[1,0,0]\n\t"
      "v_mfma_f32_16x16x32_bf16 v[196:199], a[28:31], v[20:23], v[196:199]\n\t"
      "ds_read_b128 a[68:71], v155 offset:0\n\t"
      "v_fma_mix_f32 v148, v140, v125, v129 op_sel:[1,0,0] op_sel_hi:[1,0,0]\n\t"
      "v_mfma_f32_16x16x32_bf16 v[200:203], a[32:35], v[16:19], v[200:203]\n\t"
      "ds_read_b128 a[72:75], v154 offset:32768\n\t"
      "v_fma_mix_f32 v149, v141, v127, v129 op_sel:[1,0,0] op_sel_hi:[1,0,0]\n\t"
      "v_mfma_f32_16x16x32_bf16 v[204:207], a[32:35], v[20:23], v[204:207]\n\t"
      "ds_read_b128 a[76:79], v155 offset:32768\n\t"
      "v_cvt_pk_bf16_f32 v28, v144, v145\n\t"
      "v_mfma_f32_16x16x32_bf16 v[208:211], a[36:39], v[16:19], v[208:211]\n\t"
      "ds_read_b128 a[80:83], v156 offset:0\n\t"
      "v_cvt_pk_bf16_f32 v29, v146, v147\n\t"
      "v_mfma_f32_16x16x32_bf16 v[212:215], a[36:39], v[20:23], v[212:215]\n\t"
      "ds_read_b128 a[84:87], v157 offset:0\n\t"
      "v_fma_mix_f32 v150, v142, v125, v129 op_sel:[1,0,0] op_sel_hi:[1,0,0]\n\t"
      "v_mfma_f32_16x16x32_bf16 v[216:219], a[40:43], v[16:19], v[216:219]\n\t"
      "ds_read_b128 a[88:91], v152 offset:0\n\t"
      "v_fma_mix_f32 v151, v143, v127, v129 op_sel:[1,0,0] op_sel_hi:[1,0,0]\n\t"
      "v_mfma_f32_16x16x32_bf16 v[220:223], a[40:43], v[20:23], v[220:223]\n\t"
      "ds_read_b128 a[92:95], v153 offset:0\n\t"
      "v_cvt_pk_bf16_f32 v30, v148, v149\n\t"
      "v_mfma_f32_16x16x32_bf16 v[224:227], a[44:47], v[16:19], v[224:227]\n\t"
      "ds_read_b128 a[144:147], v153 offset:34816\n\t"
      "v_cvt_pk_bf16_f32 v31, v150, v151\n\t"
      "v_mfma_f32_16x16x32_bf16 v[228:231], a[44:47], v[20:23], v[228:231]\n\t"
      "ds_read_b128 a[148:151], v154 offset:2048\n\t"
      "v_lshlrev_b32_e64 v1, 1, v15\n\t"
      "v_mfma_f32_16x16x32_bf16 v[232:235], a[48:51], v[16:19], v[232:235]\n\t"
      "ds_read_b128 a[152:155], v155 offset:2048\n\t"
      "v_sub_u32_e64 v1, v13, v1\n\t"
      "v_mfma_f32_16x16x32_bf16 v[236:239], a[48:51], v[20:23], v[236:239]\n\t"
      "ds_read_b128 a[156:159], v154 offset:34816\n\t"
      "v_lshl_add_u32 v1, v1, 1, v15\n\t"
      "v_mfma_f32_16x16x32_bf16 v[240:243], a[52:55], v[16:19], v[240:243]\n\t"
      "ds_read_b128 a[160:163], v155 offset:34816\n\t"
      "v_add_u32_e64 v1, s99, v1\n\t"
      "v_mfma_f32_16x16x32_bf16 v[244:247], a[52:55], v[20:23], v[244:247]\n\t"
      "ds_read_b128 a[164:167], v156 offset:2048\n\t"
      "v_add_u32 v2, 7168, v1\n\t"
      "v_mfma_f32_16x16x32_bf16 v[248:251], a[56:59], v[16:19], v[248:251]\n\t"
      "v_add_u32 v3, 14336, v1\n\t"
      "v_add_u32 v4, 21504, v1\n\t"
      "v_mfma_f32_16x16x32_bf16 v[252:255], a[56:59], v[20:23], v[252:255]\n\t"
      "s_waitcnt lgkmcnt(6)\n\t"
      "s_nop 0\n\t"
      "v_mfma_f32_16x16x32_bf16 a[120:123], a[60:63], v[24:27], a[120:123]\n\t"
      "ds_read_b128 a[168:171], v157 offset:2048\n\t"
      "v_mfma_f32_16x16x32_bf16 a[120:123], a[64:67], v[32:35], a[120:123]\n\t"
      "v_mfma_f32_16x16x32_bf16 a[120:123], a[68:71], v[40:43], a[120:123]\n\t"
      "ds_read_b128 a[172:175], v152 offset:2048\n\t"
      "v_mfma_f32_16x16x32_bf16 a[120:123], a[72:75], v[48:51], a[120:123]\n\t"
      "v_mfma_f32_16x16x32_bf16 a[120:123], a[76:79], v[56:59], a[120:123]\n\t"
      "ds_read_b128 a[176:179], v153 offset:2048\n\t"
      "v_mfma_f32_16x16x32_bf16 a[120:123], a[80:83], v[64:67], a[120:123]\n\t"
      "v_mfma_f32_16x16x32_bf16 a[120:123], a[84:87], v[72:75], a[120:123]\n\t"
      "ds_read_b128 a[0:3], v153 offset:40960\n\t"
      "v_mfma_f32_16x16x32_bf16 a[120:123], a[88:91], v[80:83], a[120:123]\n\t"
      "v_mfma_f32_16x16x32_bf16 a[120:123], a[92:95], v[88:91], a[120:123]\n\t"
      "ds_read_b128 a[4:7], v154 offset:8192\n\t"
      "v_mfma_f32_16x16x32_bf16 a[124:127], a[60:63], v[28:31], a[124:127]\n\t"
      "v_mfma_f32_16x16x32_bf16 a[124:127], a[64:67], v[36:39], a[124:127]\n\t"
      "ds_read_b128 a[8:11], v155 offset:8192\n\t"
      "v_mfma_f32_16x16x32_bf16 a[124:127], a[68:71], v[44:47], a[124:127]\n\t"
      "v_mfma_f32_16x16x32_bf16 a[124:127], a[72:75], v[52:55], a[124:127]\n\t"
      "ds_read_b128 a[12:15], v154 offset:40960\n\t"
      "v_mfma_f32_16x16x32_bf16 a[124:127], a[76:79], v[60:63], a[124:127]\n\t"
      "v_mfma_f32_16x16x32_bf16 a[124:127], a[80:83], v[68:71], a[124:127]\n\t"
      "ds_read_b128 a[16:19], v155 offset:40960\n\t"
      "v_mfma_f32_16x16x32_bf16 a[124:127], a[84:87], v[76:79], a[124:127]\n\t"
      "v_mfma_f32_16x16x32_bf16 a[124:127], a[88:91], v[84:87], a[124:127]\n\t"
      "ds_read_b128 a[20:23], v156 offset:8192\n\t"
      "v_mfma_f32_16x16x32_bf16 a[124:127], a[92:95], v[92:95], a[124:127]\n\t"
      "s_waitcnt lgkmcnt(6)\n\t"
      "s_nop 0\n\t"
      "v_mfma_f32_16x16x32_bf16 a[128:131], a[144:147], v[24:27], a[128:131]\n\t"
      "ds_read_b128 a[24:27], v157 offset:8192\n\t"
      "v_mfma_f32_16x16x32_bf16 a[128:131], a[148:151], v[32:35], a[128:131]\n\t"
      "s_mov_b32 s40, 0\n\t"
      "s_nop 0\n\t"
      "v_mfma_f32_16x16x32_bf16 a[128:131], a[152:155], v[40:43], a[128:131]\n\t"
      "ds_read_b128 a[28:31], v152 offset:8192\n\t"
      "v_mfma_f32_16x16x32_bf16 a[128:131], a[156:159], v[48:51], a[128:131]\n\t"
      "v_mfma_f32_16x16x32_bf16 a[128:131], a[160:163], v[56:59], a[128:131]\n\t"
      "ds_read_b128 a[32:35], v153 offset:8192\n\t"
      "v_mfma_f32_16x16x32_bf16 a[128:131], a[164:167], v[64:67], a[128:131]\n\t"
      "v_accvgpr_read_b32 v136, a120\n\t"
      "v_accvgpr_read_b32 v137, a124\n\t"
      "v_mfma_f32_16x16x32_bf16 a[128:131], a[168:171], v[72:75], a[128:131]\n\t"
      "ds_read_b128 a[60:63], v153 offset:43008\n\t"
      "v_cvt_pk_bf16_f32 v96, v136, v137\n\t"
      "v_mfma_f32_16x16x32_bf16 a[128:131], a[172:175], v[80:83], a[128:131]\n\t"
      "buffer_store_dword v96, v1, s[36:39], s40 offen sc0 sc1\n\t"
      "v_mfma_f32_16x16x32_bf16 a[128:131], a[176:179], v[88:91], a[128:131]\n\t"
      "ds_read_b128 a[64:67], v154 offset:10240\n\t"
      "v_mfma_f32_16x16x32_bf16 a[132:135], a[144:147], v[28:31], a[132:135]\n\t"
      "v_accvgpr_read_b32 v138, a121\n\t"
      "v_accvgpr_read_b32 v139, a125\n\t"
      "v_mfma_f32_16x16x32_bf16 a[132:135], a[148:151], v[36:39], a[132:135]\n\t"
      "ds_read_b128 a[68:71], v155 offset:10240\n\t"
      "v_cvt_pk_bf16_f32 v97, v138, v139\n\t"
      "v_mfma_f32_16x16x32_bf16 a[132:135], a[152:155], v[44:47], a[132:135]\n\t"
      "buffer_store_dword v97, v2, s[36:39], s40 offen sc0 sc1\n\t"
      "v_mfma_f32_16x16x32_bf16 a[132:135], a[156:159], v[52:55], a[132:135]\n\t"
      "ds_read_b128 a[72:75], v154 offset:43008\n\t"
      "v_mfma_f32_16x16x32_bf16 a[132:135], a[160:163], v[60:63], a[132:135]\n\t"
      "v_mfma_f32_16x16x32_bf16 a[132:135], a[164:167], v[68:71], a[132:135]\n\t"
      "ds_read_b128 a[76:79], v155 offset:43008\n\t"
      "v_accvgpr_read_b32 v140, a122\n\t"
      "v_accvgpr_read_b32 v141, a126\n\t"
      "v_mfma_f32_16x16x32_bf16 a[132:135], a[168:171], v[76:79], a[132:135]\n\t"
      "v_cvt_pk_bf16_f32 v98, v140, v141\n\t"
      "v_mfma_f32_16x16x32_bf16 a[132:135], a[172:175], v[84:87], a[132:135]\n\t"
      "ds_read_b128 a[80:83], v156 offset:10240\n\t"
      "buffer_store_dword v98, v3, s[36:39], s40 offen sc0 sc1\n\t"
      "v_mfma_f32_16x16x32_bf16 a[132:135], a[176:179], v[92:95], a[132:135]\n\t"
      "s_waitcnt lgkmcnt(6)\n\t"
      "s_nop 0\n\t"
      "v_mfma_f32_16x16x32_bf16 v[168:171], a[0:3], v[24:27], v[168:171]\n\t"
      "ds_read_b128 a[84:87], v157 offset:10240\n\t"
      "v_accvgpr_read_b32 v142, a123\n\t"
      "v_accvgpr_read_b32 v143, a127\n\t"
      "v_mfma_f32_16x16x32_bf16 v[168:171], a[4:7], v[32:35], v[168:171]\n\t"
      "v_cvt_pk_bf16_f32 v99, v142, v143\n\t"
      "s_mov_b32 s41, 114688\n\t"
      "v_mfma_f32_16x16x32_bf16 v[168:171], a[8:11], v[40:43], v[168:171]\n\t"
      "ds_read_b128 a[88:91], v152 offset:10240\n\t"
      "buffer_store_dword v99, v4, s[36:39], s40 offen sc0 sc1\n\t"
      "v_mfma_f32_16x16x32_bf16 v[168:171], a[12:15], v[48:51], v[168:171]\n\t"
      "v_mfma_f32_16x16x32_bf16 v[168:171], a[16:19], v[56:59], v[168:171]\n\t"
      "ds_read_b128 a[92:95], v153 offset:10240\n\t"
      "v_mfma_f32_16x16x32_bf16 v[168:171], a[20:23], v[64:67], v[168:171]\n\t"
      "v_accvgpr_read_b32 v136, a128\n\t"
      "v_accvgpr_read_b32 v137, a132\n\t"
      "v_mfma_f32_16x16x32_bf16 v[168:171], a[24:27], v[72:75], v[168:171]\n\t"
      "ds_read_b128 a[144:147], v153 offset:49152\n\t"
      "v_cvt_pk_bf16_f32 v100, v136, v137\n\t"
      "v_mfma_f32_16x16x32_bf16 v[168:171], a[28:31], v[80:83], v[168:171]\n\t"
      "buffer_store_dword v100, v1, s[36:39], s41 offen sc0 sc1\n\t"
      "v_mfma_f32_16x16x32_bf16 v[168:171], a[32:35], v[88:91], v[168:171]\n\t"
      "ds_read_b128 a[148:151], v154 offset:16384\n\t"
      "v_mfma_f32_16x16x32_bf16 v[172:175], a[0:3], v[28:31], v[172:175]\n\t"
      "v_accvgpr_read_b32 v138, a129\n\t"
      "v_accvgpr_read_b32 v139, a133\n\t"
      "v_mfma_f32_16x16x32_bf16 v[172:175], a[4:7], v[36:39], v[172:175]\n\t"
      "ds_read_b128 a[152:155], v155 offset:16384\n\t"
      "v_cvt_pk_bf16_f32 v101, v138, v139\n\t"
      "v_mfma_f32_16x16x32_bf16 v[172:175], a[8:11], v[44:47], v[172:175]\n\t"
      "buffer_store_dword v101, v2, s[36:39], s41 offen sc0 sc1\n\t"
      "v_mfma_f32_16x16x32_bf16 v[172:175], a[12:15], v[52:55], v[172:175]\n\t"
      "ds_read_b128 a[156:159], v154 offset:49152\n\t"
      "v_mfma_f32_16x16x32_bf16 v[172:175], a[16:19], v[60:63], v[172:175]\n\t"
      "v_mfma_f32_16x16x32_bf16 v[172:175], a[20:23], v[68:71], v[172:175]\n\t"
      "ds_read_b128 a[160:163], v155 offset:49152\n\t"
      "v_accvgpr_read_b32 v140, a130\n\t"
      "v_accvgpr_read_b32 v141, a134\n\t"
      "v_mfma_f32_16x16x32_bf16 v[172:175], a[24:27], v[76:79], v[172:175]\n\t"
      "v_cvt_pk_bf16_f32 v102, v140, v141\n\t"
      "v_mfma_f32_16x16x32_bf16 v[172:175], a[28:31], v[84:87], v[172:175]\n\t"
      "ds_read_b128 a[164:167], v156 offset:16384\n\t"
      "buffer_store_dword v102, v3, s[36:39], s41 offen sc0 sc1\n\t"
      "v_mfma_f32_16x16x32_bf16 v[172:175], a[32:35], v[92:95], v[172:175]\n\t"
      "s_waitcnt lgkmcnt(6)\n\t"
      "s_nop 0\n\t"
      "v_mfma_f32_16x16x32_bf16 v[176:179], a[60:63], v[24:27], v[176:179]\n\t"
      "ds_read_b128 a[168:171], v157 offset:16384\n\t"
      "v_accvgpr_read_b32 v142, a131\n\t"
      "v_accvgpr_read_b32 v143, a135\n\t"
      "v_mfma_f32_16x16x32_bf16 v[176:179], a[64:67], v[32:35], v[176:179]\n\t"
      "v_cvt_pk_bf16_f32 v103, v142, v143\n\t"
      "s_mov_b32 s40, 458752\n\t"
      "v_mfma_f32_16x16x32_bf16 v[176:179], a[68:71], v[40:43], v[176:179]\n\t"
      "ds_read_b128 a[172:175], v152 offset:16384\n\t"
      "buffer_store_dword v103, v4, s[36:39], s41 offen sc0 sc1\n\t"
      "v_mfma_f32_16x16x32_bf16 v[176:179], a[72:75], v[48:51], v[176:179]\n\t"
      "v_mfma_f32_16x16x32_bf16 v[176:179], a[76:79], v[56:59], v[176:179]\n\t"
      "ds_read_b128 a[176:179], v153 offset:16384\n\t"
      "v_mfma_f32_16x16x32_bf16 v[176:179], a[80:83], v[64:67], v[176:179]\n\t"
      "v_mfma_f32_16x16x32_bf16 v[176:179], a[84:87], v[72:75], v[176:179]\n\t"
      "ds_read_b128 a[0:3], v153 offset:51200\n\t"
      "v_cvt_pk_bf16_f32 v104, v168, v172\n\t"
      "v_mfma_f32_16x16x32_bf16 v[176:179], a[88:91], v[80:83], v[176:179]\n\t"
      "buffer_store_dword v104, v1, s[36:39], s40 offen sc0 sc1\n\t"
      "v_mfma_f32_16x16x32_bf16 v[176:179], a[92:95], v[88:91], v[176:179]\n\t"
      "ds_read_b128 a[4:7], v154 offset:18432\n\t"
      "v_mfma_f32_16x16x32_bf16 v[180:183], a[60:63], v[28:31], v[180:183]\n\t"
      "v_mfma_f32_16x16x32_bf16 v[180:183], a[64:67], v[36:39], v[180:183]\n\t"
      "ds_read_b128 a[8:11], v155 offset:18432\n\t"
      "v_cvt_pk_bf16_f32 v105, v169, v173\n\t"
      "v_mfma_f32_16x16x32_bf16 v[180:183], a[68:71], v[44:47], v[180:183]\n\t"
      "buffer_store_dword v105, v2, s[36:39], s40 offen sc0 sc1\n\t"
      "v_mfma_f32_16x16x32_bf16 v[180:183], a[72:75], v[52:55], v[180:183]\n\t"
      "ds_read_b128 a[12:15], v154 offset:51200\n\t"
      "v_mfma_f32_16x16x32_bf16 v[180:183], a[76:79], v[60:63], v[180:183]\n\t"
      "v_mfma_f32_16x16x32_bf16 v[180:183], a[80:83], v[68:71], v[180:183]\n\t"
      "ds_read_b128 a[16:19], v155 offset:51200\n\t"
      "v_mfma_f32_16x16x32_bf16 v[180:183], a[84:87], v[76:79], v[180:183]\n\t"
      "v_cvt_pk_bf16_f32 v106, v170, v174\n\t"
      "v_mfma_f32_16x16x32_bf16 v[180:183], a[88:91], v[84:87], v[180:183]\n\t"
      "ds_read_b128 a[20:23], v156 offset:18432\n\t"
      "buffer_store_dword v106, v3, s[36:39], s40 offen sc0 sc1\n\t"
      "v_mfma_f32_16x16x32_bf16 v[180:183], a[92:95], v[92:95], v[180:183]\n\t"
      "s_waitcnt lgkmcnt(6)\n\t"
      "s_nop 0\n\t"
      "v_mfma_f32_16x16x32_bf16 v[200:203], a[144:147], v[24:27], v[200:203]\n\t"
      "ds_read_b128 a[24:27], v157 offset:18432\n\t"
      "v_mfma_f32_16x16x32_bf16 v[200:203], a[148:151], v[32:35], v[200:203]\n\t"
      "v_cvt_pk_bf16_f32 v107, v171, v175\n\t"
      "s_mov_b32 s41, 573440\n\t"
      "v_mfma_f32_16x16x32_bf16 v[200:203], a[152:155], v[40:43], v[200:203]\n\t"
      "ds_read_b128 a[28:31], v152 offset:18432\n\t"
      "buffer_store_dword v107, v4, s[36:39], s40 offen sc0 sc1\n\t"
      "v_mfma_f32_16x16x32_bf16 v[200:203], a[156:159], v[48:51], v[200:203]\n\t"
      "v_mfma_f32_16x16x32_bf16 v[200:203], a[160:163], v[56:59], v[200:203]\n\t"
      "ds_read_b128 a[32:35], v153 offset:18432\n\t"
      "v_mfma_f32_16x16x32_bf16 v[200:203], a[164:167], v[64:67], v[200:203]\n\t"
      "v_mfma_f32_16x16x32_bf16 v[200:203], a[168:171], v[72:75], v[200:203]\n\t"
      "ds_read_b128 a[60:63], v153 offset:57344\n\t"
      "v_cvt_pk_bf16_f32 v96, v176, v180\n\t"
      "v_mfma_f32_16x16x32_bf16 v[200:203], a[172:175], v[80:83], v[200:203]\n\t"
      "buffer_store_dword v96, v1, s[36:39], s41 offen sc0 sc1\n\t"
      "v_mfma_f32_16x16x32_bf16 v[200:203], a[176:179], v[88:91], v[200:203]\n\t"
      "ds_read_b128 a[64:67], v154 offset:24576\n\t"
      "v_mfma_f32_16x16x32_bf16 v[204:207], a[144:147], v[28:31], v[204:207]\n\t"
      "v_mfma_f32_16x16x32_bf16 v[204:207], a[148:151], v[36:39], v[204:207]\n\t"
      "ds_read_b128 a[68:71], v155 offset:24576\n\t"
      "v_cvt_pk_bf16_f32 v97, v177, v181\n\t"
      "v_mfma_f32_16x16x32_bf16 v[204:207], a[152:155], v[44:47], v[204:207]\n\t"
      "buffer_store_dword v97, v2, s[36:39], s41 offen sc0 sc1\n\t"
      "v_mfma_f32_16x16x32_bf16 v[204:207], a[156:159], v[52:55], v[204:207]\n\t"
      "ds_read_b128 a[72:75], v154 offset:57344\n\t"
      "v_mfma_f32_16x16x32_bf16 v[204:207], a[160:163], v[60:63], v[204:207]\n\t"
      "v_mfma_f32_16x16x32_bf16 v[204:207], a[164:167], v[68:71], v[204:207]\n\t"
      "ds_read_b128 a[76:79], v155 offset:57344\n\t"
      "v_mfma_f32_16x16x32_bf16 v[204:207], a[168:171], v[76:79], v[204:207]\n\t"
      "v_cvt_pk_bf16_f32 v98, v178, v182\n\t"
      "v_mfma_f32_16x16x32_bf16 v[204:207], a[172:175], v[84:87], v[204:207]\n\t"
      "ds_read_b128 a[80:83], v156 offset:24576\n\t"
      "buffer_store_dword v98, v3, s[36:39], s41 offen sc0 sc1\n\t"
      "v_mfma_f32_16x16x32_bf16 v[204:207], a[176:179], v[92:95], v[204:207]\n\t"
      "s_waitcnt lgkmcnt(6)\n\t"
      "s_nop 0\n\t"
      "v_mfma_f32_16x16x32_bf16 v[208:211], a[0:3], v[24:27], v[208:211]\n\t"
      "ds_read_b128 a[84:87], v157 offset:24576\n\t"
      "v_mfma_f32_16x16x32_bf16 v[208:211], a[4:7], v[32:35], v[208:211]\n\t"
      "v_cvt_pk_bf16_f32 v99, v179, v183\n\t"
      "s_mov_b32 s40, 917504\n\t"
      "v_mfma_f32_16x16x32_bf16 v[208:211], a[8:11], v[40:43], v[208:211]\n\t"
      "ds_read_b128 a[88:91], v152 offset:24576\n\t"
      "buffer_store_dword v99, v4, s[36:39], s41 offen sc0 sc1\n\t"
      "v_mfma_f32_16x16x32_bf16 v[208:211], a[12:15], v[48:51], v[208:211]\n\t"
      "s_waitcnt vmcnt(16)\n\t"
      "s_barrier\n\t"
      "v_mfma_f32_16x16x32_bf16 v[208:211], a[16:19], v[56:59], v[208:211]\n\t"
      "ds_read_b128 a[92:95], v153 offset:24576\n\t"
      "v_mfma_f32_16x16x32_bf16 v[208:211], a[20:23], v[64:67], v[208:211]\n\t"
      "v_mfma_f32_16x16x32_bf16 v[208:211], a[24:27], v[72:75], v[208:211]\n\t"
      "ds_read_b128 a[144:147], v153 offset:59392\n\t"
      "v_cvt_pk_bf16_f32 v100, v200, v204\n\t"
      "v_mfma_f32_16x16x32_bf16 v[208:211], a[28:31], v[80:83], v[208:211]\n\t"
      "buffer_store_dword v100, v1, s[36:39], s40 offen sc0 sc1\n\t"
      "v_mfma_f32_16x16x32_bf16 v[208:211], a[32:35], v[88:91], v[208:211]\n\t"
      "ds_read_b128 a[148:151], v154 offset:26624\n\t"
      "v_mfma_f32_16x16x32_bf16 v[212:215], a[0:3], v[28:31], v[212:215]\n\t"
      "v_mfma_f32_16x16x32_bf16 v[212:215], a[4:7], v[36:39], v[212:215]\n\t"
      "ds_read_b128 a[152:155], v155 offset:26624\n\t"
      "v_cvt_pk_bf16_f32 v101, v201, v205\n\t"
      "v_mfma_f32_16x16x32_bf16 v[212:215], a[8:11], v[44:47], v[212:215]\n\t"
      "buffer_store_dword v101, v2, s[36:39], s40 offen sc0 sc1\n\t"
      "v_mfma_f32_16x16x32_bf16 v[212:215], a[12:15], v[52:55], v[212:215]\n\t"
      "ds_read_b128 a[156:159], v154 offset:59392\n\t"
      "v_mfma_f32_16x16x32_bf16 v[212:215], a[16:19], v[60:63], v[212:215]\n\t"
      "v_mfma_f32_16x16x32_bf16 v[212:215], a[20:23], v[68:71], v[212:215]\n\t"
      "ds_read_b128 a[160:163], v155 offset:59392\n\t"
      "v_mfma_f32_16x16x32_bf16 v[212:215], a[24:27], v[76:79], v[212:215]\n\t"
      "v_cvt_pk_bf16_f32 v102, v202, v206\n\t"
      "v_mfma_f32_16x16x32_bf16 v[212:215], a[28:31], v[84:87], v[212:215]\n\t"
      "ds_read_b128 a[164:167], v156 offset:26624\n\t"
      "buffer_store_dword v102, v3, s[36:39], s40 offen sc0 sc1\n\t"
      "v_mfma_f32_16x16x32_bf16 v[212:215], a[32:35], v[92:95], v[212:215]\n\t"
      "s_waitcnt lgkmcnt(6)\n\t"
      "s_nop 0\n\t"
      "v_mfma_f32_16x16x32_bf16 v[232:235], a[60:63], v[24:27], v[232:235]\n\t"
      "ds_read_b128 a[168:171], v157 offset:26624\n\t"
      "v_mfma_f32_16x16x32_bf16 v[232:235], a[64:67], v[32:35], v[232:235]\n\t"
      "v_cvt_pk_bf16_f32 v103, v203, v207\n\t"
      "s_mov_b32 s41, 1032192\n\t"
      "v_mfma_f32_16x16x32_bf16 v[232:235], a[68:71], v[40:43], v[232:235]\n\t"
      "ds_read_b128 a[172:175], v152 offset:26624\n\t"
      "buffer_store_dword v103, v4, s[36:39], s40 offen sc0 sc1\n\t"
      "v_mfma_f32_16x16x32_bf16 v[232:235], a[72:75], v[48:51], v[232:235]\n\t"
      "v_mfma_f32_16x16x32_bf16 v[232:235], a[76:79], v[56:59], v[232:235]\n\t"
      "ds_read_b128 a[176:179], v153 offset:26624\n\t"
      "v_mfma_f32_16x16x32_bf16 v[232:235], a[80:83], v[64:67], v[232:235]\n\t"
      "v_mfma_f32_16x16x32_bf16 v[232:235], a[84:87], v[72:75], v[232:235]\n\t"
      "ds_read_b128 a[0:3], v153 offset:36864\n\t"
      "v_cvt_pk_bf16_f32 v104, v208, v212\n\t"
      "v_mfma_f32_16x16x32_bf16 v[232:235], a[88:91], v[80:83], v[232:235]\n\t"
      "buffer_store_dword v104, v1, s[36:39], s41 offen sc0 sc1\n\t"
      "v_mfma_f32_16x16x32_bf16 v[232:235], a[92:95], v[88:91], v[232:235]\n\t"
      "ds_read_b128 a[4:7], v154 offset:4096\n\t"
      "v_mfma_f32_16x16x32_bf16 v[236:239], a[60:63], v[28:31], v[236:239]\n\t"
      "v_mfma_f32_16x16x32_bf16 v[236:239], a[64:67], v[36:39], v[236:239]\n\t"
      "ds_read_b128 a[8:11], v155 offset:4096\n\t"
      "v_cvt_pk_bf16_f32 v105, v209, v213\n\t"
      "v_mfma_f32_16x16x32_bf16 v[236:239], a[68:71], v[44:47], v[236:239]\n\t"
      "buffer_store_dword v105, v2, s[36:39], s41 offen sc0 sc1\n\t"
      "v_mfma_f32_16x16x32_bf16 v[236:239], a[72:75], v[52:55], v[236:239]\n\t"
      "ds_read_b128 a[12:15], v154 offset:36864\n\t"
      "v_mfma_f32_16x16x32_bf16 v[236:239], a[76:79], v[60:63], v[236:239]\n\t"
      "v_mfma_f32_16x16x32_bf16 v[236:239], a[80:83], v[68:71], v[236:239]\n\t"
      "ds_read_b128 a[16:19], v155 offset:36864\n\t"
      "v_mfma_f32_16x16x32_bf16 v[236:239], a[84:87], v[76:79], v[236:239]\n\t"
      "v_cvt_pk_bf16_f32 v106, v210, v214\n\t"
      "v_mfma_f32_16x16x32_bf16 v[236:239], a[88:91], v[84:87], v[236:239]\n\t"
      "ds_read_b128 a[20:23], v156 offset:4096\n\t"
      "buffer_store_dword v106, v3, s[36:39], s41 offen sc0 sc1\n\t"
      "v_mfma_f32_16x16x32_bf16 v[236:239], a[92:95], v[92:95], v[236:239]\n\t"
      "s_waitcnt lgkmcnt(6)\n\t"
      "s_nop 0\n\t"
      "v_mfma_f32_16x16x32_bf16 v[240:243], a[144:147], v[24:27], v[240:243]\n\t"
      "ds_read_b128 a[24:27], v157 offset:4096\n\t"
      "v_mfma_f32_16x16x32_bf16 v[240:243], a[148:151], v[32:35], v[240:243]\n\t"
      "v_cvt_pk_bf16_f32 v107, v211, v215\n\t"
      "s_mov_b32 s40, 1376256\n\t"
      "v_mfma_f32_16x16x32_bf16 v[240:243], a[152:155], v[40:43], v[240:243]\n\t"
      "ds_read_b128 a[28:31], v152 offset:4096\n\t"
      "buffer_store_dword v107, v4, s[36:39], s41 offen sc0 sc1\n\t"
      "v_mfma_f32_16x16x32_bf16 v[240:243], a[156:159], v[48:51], v[240:243]\n\t"
      "v_mfma_f32_16x16x32_bf16 v[240:243], a[160:163], v[56:59], v[240:243]\n\t"
      "ds_read_b128 a[32:35], v153 offset:4096\n\t"
      "v_mfma_f32_16x16x32_bf16 v[240:243], a[164:167], v[64:67], v[240:243]\n\t"
      "v_mfma_f32_16x16x32_bf16 v[240:243], a[168:171], v[72:75], v[240:243]\n\t"
      "ds_read_b128 a[60:63], v153 offset:38912\n\t"
      "v_cvt_pk_bf16_f32 v96, v232, v236\n\t"
      "v_mfma_f32_16x16x32_bf16 v[240:243], a[172:175], v[80:83], v[240:243]\n\t"
      "buffer_store_dword v96, v1, s[36:39], s40 offen sc0 sc1\n\t"
      "v_mfma_f32_16x16x32_bf16 v[240:243], a[176:179], v[88:91], v[240:243]\n\t"
      "ds_read_b128 a[64:67], v154 offset:6144\n\t"
      "v_mfma_f32_16x16x32_bf16 v[244:247], a[144:147], v[28:31], v[244:247]\n\t"
      "v_mfma_f32_16x16x32_bf16 v[244:247], a[148:151], v[36:39], v[244:247]\n\t"
      "ds_read_b128 a[68:71], v155 offset:6144\n\t"
      "v_cvt_pk_bf16_f32 v97, v233, v237\n\t"
      "v_mfma_f32_16x16x32_bf16 v[244:247], a[152:155], v[44:47], v[244:247]\n\t"
      "buffer_store_dword v97, v2, s[36:39], s40 offen sc0 sc1\n\t"
      "v_mfma_f32_16x16x32_bf16 v[244:247], a[156:159], v[52:55], v[244:247]\n\t"
      "ds_read_b128 a[72:75], v154 offset:38912\n\t"
      "v_mfma_f32_16x16x32_bf16 v[244:247], a[160:163], v[60:63], v[244:247]\n\t"
      "v_mfma_f32_16x16x32_bf16 v[244:247], a[164:167], v[68:71], v[244:247]\n\t"
      "ds_read_b128 a[76:79], v155 offset:38912\n\t"
      "v_mfma_f32_16x16x32_bf16 v[244:247], a[168:171], v[76:79], v[244:247]\n\t"
      "v_cvt_pk_bf16_f32 v98, v234, v238\n\t"
      "v_mfma_f32_16x16x32_bf16 v[244:247], a[172:175], v[84:87], v[244:247]\n\t"
      "ds_read_b128 a[80:83], v156 offset:6144\n\t"
      "buffer_store_dword v98, v3, s[36:39], s40 offen sc0 sc1\n\t"
      "v_mfma_f32_16x16x32_bf16 v[244:247], a[176:179], v[92:95], v[244:247]\n\t"
      "s_waitcnt lgkmcnt(6)\n\t"
      "s_nop 0\n\t"
      "v_mfma_f32_16x16x32_bf16 a[136:139], a[0:3], v[24:27], a[136:139]\n\t"
      "ds_read_b128 a[84:87], v157 offset:6144\n\t"
      "v_mfma_f32_16x16x32_bf16 a[136:139], a[4:7], v[32:35], a[136:139]\n\t"
      "v_cvt_pk_bf16_f32 v99, v235, v239\n\t"
      "s_mov_b32 s41, 1490944\n\t"
      "v_mfma_f32_16x16x32_bf16 a[136:139], a[8:11], v[40:43], a[136:139]\n\t"
      "ds_read_b128 a[88:91], v152 offset:6144\n\t"
      "buffer_store_dword v99, v4, s[36:39], s40 offen sc0 sc1\n\t"
      "v_mfma_f32_16x16x32_bf16 a[136:139], a[12:15], v[48:51], a[136:139]\n\t"
      "v_mfma_f32_16x16x32_bf16 a[136:139], a[16:19], v[56:59], a[136:139]\n\t"
      "ds_read_b128 a[92:95], v153 offset:6144\n\t"
      "v_mfma_f32_16x16x32_bf16 a[136:139], a[20:23], v[64:67], a[136:139]\n\t"
      "v_mfma_f32_16x16x32_bf16 a[136:139], a[24:27], v[72:75], a[136:139]\n\t"
      "ds_read_b128 a[144:147], v153 offset:45056\n\t"
      "v_cvt_pk_bf16_f32 v100, v240, v244\n\t"
      "v_mfma_f32_16x16x32_bf16 a[136:139], a[28:31], v[80:83], a[136:139]\n\t"
      "buffer_store_dword v100, v1, s[36:39], s41 offen sc0 sc1\n\t"
      "v_mfma_f32_16x16x32_bf16 a[136:139], a[32:35], v[88:91], a[136:139]\n\t"
      "ds_read_b128 a[148:151], v154 offset:12288\n\t"
      "v_mfma_f32_16x16x32_bf16 a[140:143], a[0:3], v[28:31], a[140:143]\n\t"
      "v_mfma_f32_16x16x32_bf16 a[140:143], a[4:7], v[36:39], a[140:143]\n\t"
      "ds_read_b128 a[152:155], v155 offset:12288\n\t"
      "v_cvt_pk_bf16_f32 v101, v241, v245\n\t"
      "v_mfma_f32_16x16x32_bf16 a[140:143], a[8:11], v[44:47], a[140:143]\n\t"
      "buffer_store_dword v101, v2, s[36:39], s41 offen sc0 sc1\n\t"
      "v_mfma_f32_16x16x32_bf16 a[140:143], a[12:15], v[52:55], a[140:143]\n\t"
      "ds_read_b128 a[156:159], v154 offset:45056\n\t"
      "v_mfma_f32_16x16x32_bf16 a[140:143], a[16:19], v[60:63], a[140:143]\n\t"
      "v_mfma_f32_16x16x32_bf16 a[140:143], a[20:23], v[68:71], a[140:143]\n\t"
      "ds_read_b128 a[160:163], v155 offset:45056\n\t"
      "v_mfma_f32_16x16x32_bf16 a[140:143], a[24:27], v[76:79], a[140:143]\n\t"
      "v_cvt_pk_bf16_f32 v102, v242, v246\n\t"
      "v_mfma_f32_16x16x32_bf16 a[140:143], a[28:31], v[84:87], a[140:143]\n\t"
      "ds_read_b128 a[164:167], v156 offset:12288\n\t"
      "buffer_store_dword v102, v3, s[36:39], s41 offen sc0 sc1\n\t"
      "v_mfma_f32_16x16x32_bf16 a[140:143], a[32:35], v[92:95], a[140:143]\n\t"
      "s_waitcnt lgkmcnt(6)\n\t"
      "s_nop 0\n\t"
      "v_mfma_f32_16x16x32_bf16 v[160:163], a[60:63], v[24:27], v[160:163]\n\t"
      "ds_read_b128 a[168:171], v157 offset:12288\n\t"
      "v_mfma_f32_16x16x32_bf16 v[160:163], a[64:67], v[32:35], v[160:163]\n\t"
      "v_cvt_pk_bf16_f32 v103, v243, v247\n\t"
      "s_mov_b32 s40, 229376\n\t"
      "v_mfma_f32_16x16x32_bf16 v[160:163], a[68:71], v[40:43], v[160:163]\n\t"
      "ds_read_b128 a[172:175], v152 offset:12288\n\t"
      "buffer_store_dword v103, v4, s[36:39], s41 offen sc0 sc1\n\t"
      "v_mfma_f32_16x16x32_bf16 v[160:163], a[72:75], v[48:51], v[160:163]\n\t"
      "v_mfma_f32_16x16x32_bf16 v[160:163], a[76:79], v[56:59], v[160:163]\n\t"
      "ds_read_b128 a[176:179], v153 offset:12288\n\t"
      "v_mfma_f32_16x16x32_bf16 v[160:163], a[80:83], v[64:67], v[160:163]\n\t"
      "v_accvgpr_read_b32 v136, a136\n\t"
      "v_accvgpr_read_b32 v137, a140\n\t"
      "v_mfma_f32_16x16x32_bf16 v[160:163], a[84:87], v[72:75], v[160:163]\n\t"
      "ds_read_b128 a[0:3], v153 offset:47104\n\t"
      "v_cvt_pk_bf16_f32 v104, v136, v137\n\t"
      "v_mfma_f32_16x16x32_bf16 v[160:163], a[88:91], v[80:83], v[160:163]\n\t"
      "buffer_store_dword v104, v1, s[36:39], s40 offen sc0 sc1\n\t"
      "v_mfma_f32_16x16x32_bf16 v[160:163], a[92:95], v[88:91], v[160:163]\n\t"
      "ds_read_b128 a[4:7], v154 offset:14336\n\t"
      "v_mfma_f32_16x16x32_bf16 v[164:167], a[60:63], v[28:31], v[164:167]\n\t"
      "v_accvgpr_read_b32 v138, a137\n\t"
      "v_accvgpr_read_b32 v139, a141\n\t"
      "v_mfma_f32_16x16x32_bf16 v[164:167], a[64:67], v[36:39], v[164:167]\n\t"
      "ds_read_b128 a[8:11], v155 offset:14336\n\t"
      "v_cvt_pk_bf16_f32 v105, v138, v139\n\t"
      "v_mfma_f32_16x16x32_bf16 v[164:167], a[68:71], v[44:47], v[164:167]\n\t"
      "buffer_store_dword v105, v2, s[36:39], s40 offen sc0 sc1\n\t"
      "v_mfma_f32_16x16x32_bf16 v[164:167], a[72:75], v[52:55], v[164:167]\n\t"
      "ds_read_b128 a[12:15], v154 offset:47104\n\t"
      "v_mfma_f32_16x16x32_bf16 v[164:167], a[76:79], v[60:63], v[164:167]\n\t"
      "v_mfma_f32_16x16x32_bf16 v[164:167], a[80:83], v[68:71], v[164:167]\n\t"
      "ds_read_b128 a[16:19], v155 offset:47104\n\t"
      "v_accvgpr_read_b32 v140, a138\n\t"
      "v_accvgpr_read_b32 v141, a142\n\t"
      "v_mfma_f32_16x16x32_bf16 v[164:167], a[84:87], v[76:79], v[164:167]\n\t"
      "v_cvt_pk_bf16_f32 v106, v140, v141\n\t"
      "v_mfma_f32_16x16x32_bf16 v[164:167], a[88:91], v[84:87], v[164:167]\n\t"
      "ds_read_b128 a[20:23], v156 offset:14336\n\t"
      "buffer_store_dword v106, v3, s[36:39], s40 offen sc0 sc1\n\t"
      "v_mfma_f32_16x16x32_bf16 v[164:167], a[92:95], v[92:95], v[164:167]\n\t"
      "s_waitcnt lgkmcnt(6)\n\t"
      "s_nop 0\n\t"
      "v_mfma_f32_16x16x32_bf16 v[184:187], a[144:147], v[24:27], v[184:187]\n\t"
      "ds_read_b128 a[24:27], v157 offset:14336\n\t"
      "v_accvgpr_read_b32 v142, a139\n\t"
      "v_accvgpr_read_b32 v143, a143\n\t"
      "v_mfma_f32_16x16x32_bf16 v[184:187], a[148:151], v[32:35], v[184:187]\n\t"
      "v_cvt_pk_bf16_f32 v107, v142, v143\n\t"
      "s_mov_b32 s41, 344064\n\t"
      "v_mfma_f32_16x16x32_bf16 v[184:187], a[152:155], v[40:43], v[184:187]\n\t"
      "ds_read_b128 a[28:31], v152 offset:14336\n\t"
      "buffer_store_dword v107, v4, s[36:39], s40 offen sc0 sc1\n\t"
      "v_mfma_f32_16x16x32_bf16 v[184:187], a[156:159], v[48:51], v[184:187]\n\t"
      "v_mfma_f32_16x16x32_bf16 v[184:187], a[160:163], v[56:59], v[184:187]\n\t"
      "ds_read_b128 a[32:35], v153 offset:14336\n\t"
      "v_mfma_f32_16x16x32_bf16 v[184:187], a[164:167], v[64:67], v[184:187]\n\t"
      "v_mfma_f32_16x16x32_bf16 v[184:187], a[168:171], v[72:75], v[184:187]\n\t"
      "ds_read_b128 a[60:63], v153 offset:53248\n\t"
      "v_cvt_pk_bf16_f32 v96, v160, v164\n\t"
      "v_mfma_f32_16x16x32_bf16 v[184:187], a[172:175], v[80:83], v[184:187]\n\t"
      "buffer_store_dword v96, v1, s[36:39], s41 offen sc0 sc1\n\t"
      "v_mfma_f32_16x16x32_bf16 v[184:187], a[176:179], v[88:91], v[184:187]\n\t"
      "ds_read_b128 a[64:67], v154 offset:20480\n\t"
      "v_mfma_f32_16x16x32_bf16 v[188:191], a[144:147], v[28:31], v[188:191]\n\t"
      "v_mfma_f32_16x16x32_bf16 v[188:191], a[148:151], v[36:39], v[188:191]\n\t"
      "ds_read_b128 a[68:71], v155 offset:20480\n\t"
      "v_cvt_pk_bf16_f32 v97, v161, v165\n\t"
      "v_mfma_f32_16x16x32_bf16 v[188:191], a[152:155], v[44:47], v[188:191]\n\t"
      "buffer_store_dword v97, v2, s[36:39], s41 offen sc0 sc1\n\t"
      "v_mfma_f32_16x16x32_bf16 v[188:191], a[156:159], v[52:55], v[188:191]\n\t"
      "ds_read_b128 a[72:75], v154 offset:53248\n\t"
      "v_mfma_f32_16x16x32_bf16 v[188:191], a[160:163], v[60:63], v[188:191]\n\t"
      "v_mfma_f32_16x16x32_bf16 v[188:191], a[164:167], v[68:71], v[188:191]\n\t"
      "ds_read_b128 a[76:79], v155 offset:53248\n\t"
      "v_mfma_f32_16x16x32_bf16 v[188:191], a[168:171], v[76:79], v[188:191]\n\t"
      "v_cvt_pk_bf16_f32 v98, v162, v166\n\t"
      "v_mfma_f32_16x16x32_bf16 v[188:191], a[172:175], v[84:87], v[188:191]\n\t"
      "ds_read_b128 a[80:83], v156 offset:20480\n\t"
      "buffer_store_dword v98, v3, s[36:39], s41 offen sc0 sc1\n\t"
      "v_mfma_f32_16x16x32_bf16 v[188:191], a[176:179], v[92:95], v[188:191]\n\t"
      "s_waitcnt lgkmcnt(6)\n\t"
      "s_nop 0\n\t"
      "v_mfma_f32_16x16x32_bf16 v[192:195], a[0:3], v[24:27], v[192:195]\n\t"
      "ds_read_b128 a[84:87], v157 offset:20480\n\t"
      "v_mfma_f32_16x16x32_bf16 v[192:195], a[4:7], v[32:35], v[192:195]\n\t"
      "v_cvt_pk_bf16_f32 v99, v163, v167\n\t"
      "s_mov_b32 s40, 688128\n\t"
      "v_mfma_f32_16x16x32_bf16 v[192:195], a[8:11], v[40:43], v[192:195]\n\t"
      "ds_read_b128 a[88:91], v152 offset:20480\n\t"
      "buffer_store_dword v99, v4, s[36:39], s41 offen sc0 sc1\n\t"
      "v_mfma_f32_16x16x32_bf16 v[192:195], a[12:15], v[48:51], v[192:195]\n\t"
      "v_mfma_f32_16x16x32_bf16 v[192:195], a[16:19], v[56:59], v[192:195]\n\t"
      "ds_read_b128 a[92:95], v153 offset:20480\n\t"
      "v_mfma_f32_16x16x32_bf16 v[192:195], a[20:23], v[64:67], v[192:195]\n\t"
      "v_mfma_f32_16x16x32_bf16 v[192:195], a[24:27], v[72:75], v[192:195]\n\t"
      "ds_read_b128 a[144:147], v153 offset:55296\n\t"
      "v_cvt_pk_bf16_f32 v100, v184, v188\n\t"
      "v_mfma_f32_16x16x32_bf16 v[192:195], a[28:31], v[80:83], v[192:195]\n\t"
      "buffer_store_dword v100, v1, s[36:39], s40 offen sc0 sc1\n\t"
      "v_mfma_f32_16x16x32_bf16 v[192:195], a[32:35], v[88:91], v[192:195]\n\t"
      "ds_read_b128 a[148:151], v154 offset:22528\n\t"
      "v_mfma_f32_16x16x32_bf16 v[196:199], a[0:3], v[28:31], v[196:199]\n\t"
      "v_mfma_f32_16x16x32_bf16 v[196:199], a[4:7], v[36:39], v[196:199]\n\t"
      "ds_read_b128 a[152:155], v155 offset:22528\n\t"
      "v_cvt_pk_bf16_f32 v101, v185, v189\n\t"
      "v_mfma_f32_16x16x32_bf16 v[196:199], a[8:11], v[44:47], v[196:199]\n\t"
      "buffer_store_dword v101, v2, s[36:39], s40 offen sc0 sc1\n\t"
      "v_mfma_f32_16x16x32_bf16 v[196:199], a[12:15], v[52:55], v[196:199]\n\t"
      "ds_read_b128 a[156:159], v154 offset:55296\n\t"
      "v_mfma_f32_16x16x32_bf16 v[196:199], a[16:19], v[60:63], v[196:199]\n\t"
      "v_mfma_f32_16x16x32_bf16 v[196:199], a[20:23], v[68:71], v[196:199]\n\t"
      "ds_read_b128 a[160:163], v155 offset:55296\n\t"
      "v_mfma_f32_16x16x32_bf16 v[196:199], a[24:27], v[76:79], v[196:199]\n\t"
      "v_cvt_pk_bf16_f32 v102, v186, v190\n\t"
      "v_mfma_f32_16x16x32_bf16 v[196:199], a[28:31], v[84:87], v[196:199]\n\t"
      "ds_read_b128 a[164:167], v156 offset:22528\n\t"
      "buffer_store_dword v102, v3, s[36:39], s40 offen sc0 sc1\n\t"
      "v_mfma_f32_16x16x32_bf16 v[196:199], a[32:35], v[92:95], v[196:199]\n\t"
      "s_waitcnt lgkmcnt(6)\n\t"
      "s_nop 0\n\t"
      "v_mfma_f32_16x16x32_bf16 v[216:219], a[60:63], v[24:27], v[216:219]\n\t"
      "ds_read_b128 a[168:171], v157 offset:22528\n\t"
      "v_mfma_f32_16x16x32_bf16 v[216:219], a[64:67], v[32:35], v[216:219]\n\t"
      "v_cvt_pk_bf16_f32 v103, v187, v191\n\t"
      "s_mov_b32 s41, 802816\n\t"
      "v_mfma_f32_16x16x32_bf16 v[216:219], a[68:71], v[40:43], v[216:219]\n\t"
      "ds_read_b128 a[172:175], v152 offset:22528\n\t"
      "buffer_store_dword v103, v4, s[36:39], s40 offen sc0 sc1\n\t"
      "v_mfma_f32_16x16x32_bf16 v[216:219], a[72:75], v[48:51], v[216:219]\n\t"
      "v_mfma_f32_16x16x32_bf16 v[216:219], a[76:79], v[56:59], v[216:219]\n\t"
      "ds_read_b128 a[176:179], v153 offset:22528\n\t"
      "v_mfma_f32_16x16x32_bf16 v[216:219], a[80:83], v[64:67], v[216:219]\n\t"
      "v_mfma_f32_16x16x32_bf16 v[216:219], a[84:87], v[72:75], v[216:219]\n\t"
      "ds_read_b128 a[0:3], v153 offset:61440\n\t"
      "v_cvt_pk_bf16_f32 v104, v192, v196\n\t"
      "v_mfma_f32_16x16x32_bf16 v[216:219], a[88:91], v[80:83], v[216:219]\n\t"
      "buffer_store_dword v104, v1, s[36:39], s41 offen sc0 sc1\n\t"
      "v_mfma_f32_16x16x32_bf16 v[216:219], a[92:95], v[88:91], v[216:219]\n\t"
      "ds_read_b128 a[4:7], v154 offset:28672\n\t"
      "v_mfma_f32_16x16x32_bf16 v[220:223], a[60:63], v[28:31], v[220:223]\n\t"
      "v_mfma_f32_16x16x32_bf16 v[220:223], a[64:67], v[36:39], v[220:223]\n\t"
      "ds_read_b128 a[8:11], v155 offset:28672\n\t"
      "v_cvt_pk_bf16_f32 v105, v193, v197\n\t"
      "v_mfma_f32_16x16x32_bf16 v[220:223], a[68:71], v[44:47], v[220:223]\n\t"
      "buffer_store_dword v105, v2, s[36:39], s41 offen sc0 sc1\n\t"
      "v_mfma_f32_16x16x32_bf16 v[220:223], a[72:75], v[52:55], v[220:223]\n\t"
      "ds_read_b128 a[12:15], v154 offset:61440\n\t"
      "v_mfma_f32_16x16x32_bf16 v[220:223], a[76:79], v[60:63], v[220:223]\n\t"
      "v_mfma_f32_16x16x32_bf16 v[220:223], a[80:83], v[68:71], v[220:223]\n\t"
      "ds_read_b128 a[16:19], v155 offset:61440\n\t"
      "v_mfma_f32_16x16x32_bf16 v[220:223], a[84:87], v[76:79], v[220:223]\n\t"
      "v_cvt_pk_bf16_f32 v106, v194, v198\n\t"
      "v_mfma_f32_16x16x32_bf16 v[220:223], a[88:91], v[84:87], v[220:223]\n\t"
      "ds_read_b128 a[20:23], v156 offset:28672\n\t"
      "buffer_store_dword v106, v3, s[36:39], s41 offen sc0 sc1\n\t"
      "v_mfma_f32_16x16x32_bf16 v[220:223], a[92:95], v[92:95], v[220:223]\n\t"
      "s_waitcnt lgkmcnt(6)\n\t"
      "s_nop 0\n\t"
      "v_mfma_f32_16x16x32_bf16 v[224:227], a[144:147], v[24:27], v[224:227]\n\t"
      "ds_read_b128 a[24:27], v157 offset:28672\n\t"
      "v_mfma_f32_16x16x32_bf16 v[224:227], a[148:151], v[32:35], v[224:227]\n\t"
      "v_cvt_pk_bf16_f32 v107, v195, v199\n\t"
      "s_mov_b32 s40, 1146880\n\t"
      "v_mfma_f32_16x16x32_bf16 v[224:227], a[152:155], v[40:43], v[224:227]\n\t"
      "ds_read_b128 a[28:31], v152 offset:28672\n\t"
      "buffer_store_dword v107, v4, s[36:39], s41 offen sc0 sc1\n\t"
      "v_mfma_f32_16x16x32_bf16 v[224:227], a[156:159], v[48:51], v[224:227]\n\t"
      "v_mfma_f32_16x16x32_bf16 v[224:227], a[160:163], v[56:59], v[224:227]\n\t"
      "ds_read_b128 a[32:35], v153 offset:28672\n\t"
      "v_mfma_f32_16x16x32_bf16 v[224:227], a[164:167], v[64:67], v[224:227]\n\t"
      "v_mfma_f32_16x16x32_bf16 v[224:227], a[168:171], v[72:75], v[224:227]\n\t"
      "v_cvt_pk_bf16_f32 v96, v216, v220\n\t"
      "v_mfma_f32_16x16x32_bf16 v[224:227], a[172:175], v[80:83], v[224:227]\n\t"
      "buffer_store_dword v96, v1, s[36:39], s40 offen sc0 sc1\n\t"
      "v_mfma_f32_16x16x32_bf16 v[224:227], a[176:179], v[88:91], v[224:227]\n\t"
      "v_mfma_f32_16x16x32_bf16 v[228:231], a[144:147], v[28:31], v[228:231]\n\t"
      "v_mfma_f32_16x16x32_bf16 v[228:231], a[148:151], v[36:39], v[228:231]\n\t"
      "v_cvt_pk_bf16_f32 v97, v217, v221\n\t"
      "v_mfma_f32_16x16x32_bf16 v[228:231], a[152:155], v[44:47], v[228:231]\n\t"
      "buffer_store_dword v97, v2, s[36:39], s40 offen sc0 sc1\n\t"
      "v_mfma_f32_16x16x32_bf16 v[228:231], a[156:159], v[52:55], v[228:231]\n\t"
      "v_mfma_f32_16x16x32_bf16 v[228:231], a[160:163], v[60:63], v[228:231]\n\t"
      "v_mfma_f32_16x16x32_bf16 v[228:231], a[164:167], v[68:71], v[228:231]\n\t"
      "v_mfma_f32_16x16x32_bf16 v[228:231], a[168:171], v[76:79], v[228:231]\n\t"
      "v_cvt_pk_bf16_f32 v98, v218, v222\n\t"
      "v_mfma_f32_16x16x32_bf16 v[228:231], a[172:175], v[84:87], v[228:231]\n\t"
      "buffer_store_dword v98, v3, s[36:39], s40 offen sc0 sc1\n\t"
      "v_mfma_f32_16x16x32_bf16 v[228:231], a[176:179], v[92:95], v[228:231]\n\t"
      "s_waitcnt lgkmcnt(0)\n\t"
      "s_nop 0\n\t"
      "v_mfma_f32_16x16x32_bf16 v[248:251], a[0:3], v[24:27], v[248:251]\n\t"
      "v_mfma_f32_16x16x32_bf16 v[248:251], a[4:7], v[32:35], v[248:251]\n\t"
      "v_cvt_pk_bf16_f32 v99, v219, v223\n\t"
      "s_mov_b32 s41, 1261568\n\t"
      "v_mfma_f32_16x16x32_bf16 v[248:251], a[8:11], v[40:43], v[248:251]\n\t"
      "buffer_store_dword v99, v4, s[36:39], s40 offen sc0 sc1\n\t"
      "v_mfma_f32_16x16x32_bf16 v[248:251], a[12:15], v[48:51], v[248:251]\n\t"
      "v_mfma_f32_16x16x32_bf16 v[248:251], a[16:19], v[56:59], v[248:251]\n\t"
      "v_mfma_f32_16x16x32_bf16 v[248:251], a[20:23], v[64:67], v[248:251]\n\t"
      "v_mfma_f32_16x16x32_bf16 v[248:251], a[24:27], v[72:75], v[248:251]\n\t"
      "v_cvt_pk_bf16_f32 v100, v224, v228\n\t"
      "v_mfma_f32_16x16x32_bf16 v[248:251], a[28:31], v[80:83], v[248:251]\n\t"
      "buffer_store_dword v100, v1, s[36:39], s41 offen sc0 sc1\n\t"
      "v_mfma_f32_16x16x32_bf16 v[248:251], a[32:35], v[88:91], v[248:251]\n\t"
      "v_cvt_pk_bf16_f32 v101, v225, v229\n\t"
      "v_mfma_f32_16x16x32_bf16 v[252:255], a[0:3], v[28:31], v[252:255]\n\t"
      "buffer_store_dword v101, v2, s[36:39], s41 offen sc0 sc1\n\t"
      "v_mfma_f32_16x16x32_bf16 v[252:255], a[4:7], v[36:39], v[252:255]\n\t"
      "v_cvt_pk_bf16_f32 v102, v226, v230\n\t"
      "v_mfma_f32_16x16x32_bf16 v[252:255], a[8:11], v[44:47], v[252:255]\n\t"
      "buffer_store_dword v102, v3, s[36:39], s41 offen sc0 sc1\n\t"
      "v_mfma_f32_16x16x32_bf16 v[252:255], a[12:15], v[52:55], v[252:255]\n\t"
      "v_cvt_pk_bf16_f32 v103, v227, v231\n\t"
      "v_mfma_f32_16x16x32_bf16 v[252:255], a[16:19], v[60:63], v[252:255]\n\t"
      "buffer_store_dword v103, v4, s[36:39], s41 offen sc0 sc1\n\t"
      "v_mfma_f32_16x16x32_bf16 v[252:255], a[20:23], v[68:71], v[252:255]\n\t"
      "v_mfma_f32_16x16x32_bf16 v[252:255], a[24:27], v[76:79], v[252:255]\n\t"
      "v_mfma_f32_16x16x32_bf16 v[252:255], a[28:31], v[84:87], v[252:255]\n\t"
      "v_mfma_f32_16x16x32_bf16 v[252:255], a[32:35], v[92:95], v[252:255]\n\t"
      "s_nop 7\n\t"
      "s_nop 1\n\t"
      "s_mov_b32 s40, 1605632\n\t"
      "v_cvt_pk_bf16_f32 v104, v248, v252\n\t"
      "v_cvt_pk_bf16_f32 v105, v249, v253\n\t"
      "v_cvt_pk_bf16_f32 v106, v250, v254\n\t"
      "v_cvt_pk_bf16_f32 v107, v251, v255\n\t"
      "buffer_store_dword v104, v1, s[36:39], s40 offen sc0 sc1\n\t"
      "buffer_store_dword v105, v2, s[36:39], s40 offen sc0 sc1\n\t"
      "buffer_store_dword v106, v3, s[36:39], s40 offen sc0 sc1\n\t"
      "buffer_store_dword v107, v4, s[36:39], s40 offen sc0 sc1\n\t"
      "LEND%=:\n\t"
      :
      : [ka] "s"(ka), [bid] "s"(bid), [tid] "v"(tid)
      : "memory", "scc", "vcc", "s4", "s5", "s6", "s7", "s8", "s9", "s10", "s11", "s12", "s13", "s14", "s15", "s16", "s17", "s18", "s19", "s20", "s21", "s22", "s23", "s24", "s25", "s26", "s27", "s28", "s29", "s30", "s31", "s36", "s37", "s38", "s39", "s40", "s41", "s42", "s43", "s44", "s45", "s46", "s47", "s48", "s49", "s50", "s51", "s52", "s53", "s54", "s55", "s56", "s57", "s58", "s59", "s60", "s61", "s62", "s63", "s64", "s65", "s66", "s67", "s68", "s69", "s70", "s71", "s72", "s73", "s74", "s75", "s76", "s77", "s78", "s79", "s80", "s81", "s82", "s83", "s84", "s85", "s86", "s87", "s88", "s89", "s90", "s91", "s92", "s93", "s94", "s95", "s96", "s97", "s98", "s99", "v1", "v2", "v3", "v4", "v5", "v6", "v7", "v8", "v9", "v10", "v11", "v12", "v13", "v14", "v15", "v16", "v17", "v18", "v19", "v20", "v21", "v22", "v23", "v24", "v25", "v26", "v27", "v28", "v29", "v30", "v31", "v32", "v33", "v34", "v35", "v36", "v37", "v38", "v39", "v40", "v41", "v42", "v43", "v44", "v45", "v46", "v47", "v48", "v49", "v50", "v51", "v52", "v53", "v54", "v55", "v56", "v57", "v58", "v59", "v60", "v61", "v62", "v63", "v64", "v65", "v66", "v67", "v68", "v69", "v70", "v71", "v72", "v73", "v74", "v75", "v76", "v77", "v78", "v79", "v80", "v81", "v82", "v83", "v84", "v85", "v86", "v87", "v88", "v89", "v90", "v91", "v92", "v93", "v94", "v95", "v96", "v97", "v98", "v99", "v100", "v101", "v102", "v103", "v104", "v105", "v106", "v107", "v108", "v109", "v110", "v111", "v112", "v113", "v114", "v115", "v116", "v117", "v118", "v119", "v120", "v121", "v122", "v123", "v124", "v125", "v126", "v127", "v128", "v129", "v130", "v131", "v132", "v133", "v134", "v135", "v136", "v137", "v138", "v139", "v140", "v141", "v142", "v143", "v144", "v145", "v146", "v147", "v148", "v149", "v150", "v151", "v152", "v153", "v154", "v155", "v156", "v157", "v158", "v159", "v160", "v161", "v162", "v163", "v164", "v165", "v166", "v167", "v168", "v169", "v170", "v171", "v172", "v173", "v174", "v175", "v176", "v177", "v178", "v179", "v180", "v181", "v182", "v183", "v184", "v185", "v186", "v187", "v188", "v189", "v190", "v191", "v192", "v193", "v194", "v195", "v196", "v197", "v198", "v199", "v200", "v201", "v202", "v203", "v204", "v205", "v206", "v207", "v208", "v209", "v210", "v211", "v212", "v213", "v214", "v215", "v216", "v217", "v218", "v219", "v220", "v221", "v222", "v223", "v224", "v225", "v226", "v227", "v228", "v229", "v230", "v231", "v232", "v233", "v234", "v235", "v236", "v237", "v238", "v239", "v240", "v241", "v242", "v243", "v244", "v245", "v246", "v247", "v248", "v249", "v250", "v251", "v252", "v253", "v254", "v255", "a0", "a1", "a2", "a3", "a4", "a5", "a6", "a7", "a8", "a9", "a10", "a11", "a12", "a13", "a14", "a15", "a16", "a17", "a18", "a19", "a20", "a21", "a22", "a23", "a24", "a25", "a26", "a27", "a28", "a29", "a30", "a31", "a32", "a33", "a34", "a35", "a36", "a37", "a38", "a39", "a40", "a41", "a42", "a43", "a44", "a45", "a46", "a47", "a48", "a49", "a50", "a51", "a52", "a53", "a54", "a55", "a56", "a57", "a58", "a59", "a60", "a61", "a62", "a63", "a64", "a65", "a66", "a67", "a68", "a69", "a70", "a71", "a72", "a73", "a74", "a75", "a76", "a77", "a78", "a79", "a80", "a81", "a82", "a83", "a84", "a85", "a86", "a87", "a88", "a89", "a90", "a91", "a92", "a93", "a94", "a95", "a96", "a97", "a98", "a99", "a100", "a101", "a102", "a103", "a104", "a105", "a106", "a107", "a108", "a109", "a110", "a111", "a112", "a113", "a114", "a115", "a116", "a117", "a118", "a119", "a120", "a121", "a122", "a123", "a124", "a125", "a126", "a127", "a128", "a129", "a130", "a131", "a132", "a133", "a134", "a135", "a136", "a137", "a138", "a139", "a140", "a141", "a142", "a143", "a144", "a145", "a146", "a147", "a148", "a149", "a150", "a151", "a152", "a153", "a154", "a155", "a156", "a157", "a158", "a159", "a160", "a161", "a162", "a163", "a164", "a165", "a166", "a167", "a168", "a169", "a170", "a171", "a172", "a173", "a174", "a175", "a176", "a177", "a178", "a179");
}

extern "C" void kernel_launch(void* const* d_in, const int* in_sizes, int n_in,
                              void* d_out, int out_size, void* d_ws, size_t ws_size,
                              hipStream_t stream) {
  (void)in_sizes; (void)n_in; (void)out_size; (void)d_ws; (void)ws_size;
  (void)hipFuncSetAttribute((const void*)gptq_gemm, hipFuncAttributeMaxDynamicSharedMemorySize, LDS_BYTES);
  gptq_gemm<<<NBLK, 256, LDS_BYTES, stream>>>((const uint16_t*)d_in[0], (const unsigned*)d_in[1], (const uint16_t*)d_in[2],
                                              (const uint16_t*)d_in[3], (uint16_t*)d_out);
}
```
